```python
import torch

def _softmax(x, axis=-1):
    return torch.softmax(x, dim=axis)
def _gelu(x):
    return torch.nn.functional.gelu(x, approximate="tanh")
def _silu(x):
    return torch.nn.functional.silu(x)
def _sigmoid(x):
    return torch.sigmoid(x)
def _layernorm(x, axis=-1, eps=1e-5):
    m = x.mean(dim=axis, keepdim=True)
    v = x.var(dim=axis, keepdim=True, unbiased=False)
    return (x - m) / torch.sqrt(v + eps)
def _scatter_add(tgt, idx, src, axis=0):
    return tgt.clone().index_add_(axis, idx, src)
def _segment_sum(src, seg, n):
    out = torch.zeros((n,) + src.shape[1:], dtype=src.dtype)
    return out.index_add_(0, seg, src)
def _pad(x, pw):
    flat = tuple(v for pair in reversed(pw) for v in pair)
    return torch.nn.functional.pad(x, flat)

kernel_name = 'standalone__gqa_block_cached__sampling'

def setup_inputs():

    _w = (lambda a: (lambda t: t.to(torch.bfloat16) if t.is_floating_point() else t)(torch.as_tensor(a)))
    B, Sq, D = 8, 4, 512
    Hq, Hkv, Sk, Dh = 32, 8, 2048, 128
    assert Hq % Hkv == 0, (Hq, Hkv)
    return {
        "x": _w((lambda *s: torch.randn(*s, dtype=torch.bfloat16) * 0.8)(B, Sq, D)),
        "k_cache": _w((lambda *s: torch.randn(*s, dtype=torch.bfloat16) * 0.8)(B, Hkv, Sk, Dh)),
        "v_cache": _w((lambda *s: torch.randn(*s, dtype=torch.bfloat16) * 0.05)(B, Hkv, Sk, Dh)),
        "wq": _w((lambda *s: torch.randn(*s, dtype=torch.bfloat16) * 0.8)(D, Hq * Dh) * (D ** -0.5)),
        "wo": _w((lambda *s: torch.randn(*s, dtype=torch.bfloat16) * 0.05)(Hq * Dh, D) * ((Hq * Dh) ** -0.5)),
    }

def reference(x, k_cache, v_cache, wq, wo):
    B, Sq, D = x.shape
    Hq, Dh = 32, 128
    Hkv = k_cache.shape[1]
    rep = Hq // Hkv
    q = (lambda x, *d: x.permute(*d))((x @ wq).reshape(B, Sq, Hq, Dh), 0, 2, 1, 3)
    k = (lambda x, n, axis: torch.repeat_interleave(x, n, dim=axis))(k_cache, rep, axis=1)
    v = (lambda x, n, axis: torch.repeat_interleave(x, n, dim=axis))(v_cache, rep, axis=1)
    scores = torch.einsum("bhqd,bhkd->bhqk", q, k) * (Dh ** -0.5)
    p = _softmax(scores, axis=-1)
    o = torch.einsum("bhqk,bhkd->bhqd", p, v)
    return (lambda x, *d: x.permute(*d))(o, 0, 2, 1, 3).reshape(B, Sq, Hq * Dh) @ wo
```

```cpp
#include <hip/hip_runtime.h>
#include <hip/hip_bf16.h>
#include <stdint.h>

typedef unsigned v4u __attribute__((ext_vector_type(4)));
typedef float f32x4 __attribute__((ext_vector_type(4)));
typedef short bf16x8 __attribute__((ext_vector_type(8)));

constexpr int B = 8, SQ = 4, D = 512, HQ = 32, HKV = 8, SK = 2048, DH = 128;
constexpr int NSPLIT = 4;

__device__ __forceinline__ f32x4 mfma(v4u a, v4u b, f32x4 c) {
  return __builtin_amdgcn_mfma_f32_16x16x32_bf16(__builtin_bit_cast(bf16x8, a), __builtin_bit_cast(bf16x8, b), c, 0, 0, 0);
}
__device__ __forceinline__ unsigned cvtpk(float lo, float hi) {
  unsigned r; asm volatile("v_cvt_pk_bf16_f32 %0, %1, %2" : "=v"(r) : "v"(lo), "v"(hi)); return r;
}
__device__ __forceinline__ void transpose8(const v4u (&in)[8], v4u (&out)[8]) {
#pragma unroll
  for (int w = 0; w < 4; ++w) {
#pragma unroll
    for (int u = 0; u < 4; ++u) {
      out[2 * w][u] = __builtin_amdgcn_perm(in[2 * u + 1][w], in[2 * u][w], 0x05040100u);
      out[2 * w + 1][u] = __builtin_amdgcn_perm(in[2 * u + 1][w], in[2 * u][w], 0x07060302u);
    }
  }
}

__global__ void __launch_bounds__(512) k_qproj(const uint16_t* __restrict__ x, const uint16_t* __restrict__ wq, uint16_t* __restrict__ q) {
  __shared__ float red[8][4][128];
  const int h = blockIdx.x & 31, b = blockIdx.x >> 5;
  const int tid = threadIdx.x, lane = tid & 63, w = tid >> 6, g = lane >> 4, c = lane & 15;
  f32x4 acc[8];
#pragma unroll
  for (int j = 0; j < 8; ++j) acc[j] = f32x4{0.f, 0.f, 0.f, 0.f};
#pragma unroll
  for (int ks = 0; ks < 2; ++ks) {
    const int k0 = 64 * w + 32 * ks;
    v4u in[8], fr[8];
#pragma unroll
    for (int i = 0; i < 8; ++i) in[i] = *(const v4u*)(wq + (size_t)(k0 + 8 * g + i) * (HQ * DH) + h * DH + 8 * c);
    v4u xf = *(const v4u*)(x + (size_t)(b * SQ + (c & 3)) * D + k0 + 8 * g);
    transpose8(in, fr);
#pragma unroll
    for (int j = 0; j < 8; ++j) acc[j] = mfma(xf, fr[j], acc[j]);
  }
  if (g == 0) {
#pragma unroll
    for (int j = 0; j < 8; ++j)
#pragma unroll
      for (int r = 0; r < 4; ++r) red[w][r][8 * c + j] = acc[j][r];
  }
  __syncthreads();
  {
    const int r = tid >> 7, n = tid & 127;
    float s = 0.f;
#pragma unroll
    for (int ww = 0; ww < 8; ++ww) s += red[ww][r][n];
    q[((size_t)(b * HQ + h) * SQ + r) * DH + n] = (uint16_t)(cvtpk(s, 0.f) & 0xffffu);
  }
}

__global__ void __launch_bounds__(512) k_attn(const uint16_t* __restrict__ q, const uint16_t* __restrict__ kc, const uint16_t* __restrict__ vc,
                                               float* __restrict__ po, float* __restrict__ pml) {
  __shared__ __attribute__((aligned(16))) float red[8][16][132];
  __shared__ float sml[8][16][2];
  const int kvh = blockIdx.x & 7, s = (blockIdx.x >> 3) & 3, b = blockIdx.x >> 5;
  const int tid = threadIdx.x, lane = tid & 63, w = tid >> 6, g = lane >> 4, c = lane & 15;
  const size_t base = ((size_t)(b * HKV + kvh) * SK + s * 512 + w * 64) * DH;
  const uint16_t* kp = kc + base;
  const uint16_t* vp = vc + base;
  v4u kf[4][4], vf[2][8];
#pragma unroll
  for (int t = 0; t < 4; ++t)
#pragma unroll
    for (int st = 0; st < 4; ++st) kf[t][st] = *(const v4u*)(kp + (size_t)(16 * t + c) * DH + 32 * st + 8 * g);
#pragma unroll
  for (int bl = 0; bl < 2; ++bl)
#pragma unroll
    for (int i = 0; i < 8; ++i) vf[bl][i] = *(const v4u*)(vp + (size_t)(32 * bl + (i >> 2) * 16 + 4 * g + (i & 3)) * DH + 8 * c);
  v4u qf[4];
#pragma unroll
  for (int st = 0; st < 4; ++st) qf[st] = *(const v4u*)(q + ((size_t)(b * HQ + kvh * 4) * SQ + c) * DH + 32 * st + 8 * g);
  f32x4 sc[4];
#pragma unroll
  for (int t = 0; t < 4; ++t) {
    sc[t] = f32x4{0.f, 0.f, 0.f, 0.f};
#pragma unroll
    for (int st = 0; st < 4; ++st) sc[t] = mfma(kf[t][st], qf[st], sc[t]);
  }
  constexpr float C2 = 0.088388347648318440f * 1.4426950408889634f;
  float m = -INFINITY;
#pragma unroll
  for (int t = 0; t < 4; ++t)
#pragma unroll
    for (int r = 0; r < 4; ++r) m = fmaxf(m, sc[t][r]);
  m = fmaxf(m, __shfl_xor(m, 16));
  m = fmaxf(m, __shfl_xor(m, 32));
  const float ms = m * C2;
  float l = 0.f;
#pragma unroll
  for (int t = 0; t < 4; ++t)
#pragma unroll
    for (int r = 0; r < 4; ++r) { sc[t][r] = __builtin_amdgcn_exp2f(sc[t][r] * C2 - ms); l += sc[t][r]; }
  l += __shfl_xor(l, 16);
  l += __shfl_xor(l, 32);
  f32x4 acc[8];
#pragma unroll
  for (int j = 0; j < 8; ++j) acc[j] = f32x4{0.f, 0.f, 0.f, 0.f};
#pragma unroll
  for (int bl = 0; bl < 2; ++bl) {
    v4u pf = {cvtpk(sc[2 * bl][0], sc[2 * bl][1]), cvtpk(sc[2 * bl][2], sc[2 * bl][3]), cvtpk(sc[2 * bl + 1][0], sc[2 * bl + 1][1]), cvtpk(sc[2 * bl + 1][2], sc[2 * bl + 1][3])};
    v4u fr[8];
    transpose8(vf[bl], fr);
#pragma unroll
    for (int j = 0; j < 8; ++j) acc[j] = mfma(fr[j], pf, acc[j]);
  }
#pragma unroll
  for (int r = 0; r < 4; ++r) {
    *(f32x4*)&red[w][c][32 * g + 8 * r] = f32x4{acc[0][r], acc[1][r], acc[2][r], acc[3][r]};
    *(f32x4*)&red[w][c][32 * g + 8 * r + 4] = f32x4{acc[4][r], acc[5][r], acc[6][r], acc[7][r]};
  }
  if (g == 0) { sml[w][c][0] = ms; sml[w][c][1] = l; }
  __syncthreads();
  {
    const int row = tid >> 5, d0 = (tid & 31) * 4;
    float M = -INFINITY;
#pragma unroll
    for (int ww = 0; ww < 8; ++ww) M = fmaxf(M, sml[ww][row][0]);
    f32x4 o = {0.f, 0.f, 0.f, 0.f};
    float L = 0.f;
#pragma unroll
    for (int ww = 0; ww < 8; ++ww) {
      const float f = __builtin_amdgcn_exp2f(sml[ww][row][0] - M);
      L += f * sml[ww][row][1];
      o += f * *(const f32x4*)&red[ww][row][d0];
    }
    const size_t slot = ((size_t)(b * HKV + kvh) * 16 + row) * NSPLIT + s;
    *(f32x4*)(po + slot * DH + d0) = o;
    if ((tid & 31) == 0) { pml[slot * 2] = M; pml[slot * 2 + 1] = L; }
  }
}

__global__ void __launch_bounds__(512) k_out(const float* __restrict__ po, const float* __restrict__ pml, const uint16_t* __restrict__ wo, uint16_t* __restrict__ out) {
  __shared__ float o_s[4][4096];
  __shared__ float red[32][4][16];
  const int cb = blockIdx.x & 31, b = blockIdx.x >> 5;
  const int tid = threadIdx.x;
  for (int idx = tid; idx < 128 * 32; idx += 512) {
    const int row = idx >> 5, d0 = (idx & 31) * 4;
    const size_t slot = ((size_t)b * 128 + row) * NSPLIT;
    float M = -INFINITY;
#pragma unroll
    for (int s = 0; s < NSPLIT; ++s) M = fmaxf(M, pml[(slot + s) * 2]);
    float L = 0.f;
    f32x4 o = {0.f, 0.f, 0.f, 0.f};
#pragma unroll
    for (int s = 0; s < NSPLIT; ++s) {
      const float f = __builtin_amdgcn_exp2f(pml[(slot + s) * 2] - M);
      L += f * pml[(slot + s) * 2 + 1];
      o += f * *(const f32x4*)(po + (slot + s) * DH + d0);
    }
    const float inv = 1.f / L;
    const int h = row >> 2, pos = row & 3;
#pragma unroll
    for (int e = 0; e < 4; ++e) {
      const unsigned r = cvtpk(o[e] * inv, 0.f);
      o_s[pos][h * 128 + d0 + e] = __builtin_bit_cast(float, r << 16);
    }
  }
  __syncthreads();
  const int kg = tid >> 4, col = tid & 15;
  float a0 = 0.f, a1 = 0.f, a2 = 0.f, a3 = 0.f;
  for (int k = kg * 128; k < kg * 128 + 128; ++k) {
    const float wv = __builtin_bit_cast(float, (unsigned)wo[(size_t)k * D + cb * 16 + col] << 16);
    a0 += o_s[0][k] * wv; a1 += o_s[1][k] * wv; a2 += o_s[2][k] * wv; a3 += o_s[3][k] * wv;
  }
  red[kg][0][col] = a0; red[kg][1][col] = a1; red[kg][2][col] = a2; red[kg][3][col] = a3;
  __syncthreads();
  if (tid < 64) {
    const int pos = tid >> 4, cc = tid & 15;
    float s = 0.f;
    for (int k = 0; k < 32; ++k) s += red[k][pos][cc];
    out[(size_t)(b * SQ + pos) * D + cb * 16 + cc] = (uint16_t)(cvtpk(s, 0.f) & 0xffffu);
  }
}

extern "C" void kernel_launch(void* const* d_in, const int* in_sizes, int n_in, void* d_out, int out_size, void* d_ws, size_t ws_size,
                              hipStream_t stream) {
  const uint16_t* x = (const uint16_t*)d_in[0];
  const uint16_t* kc = (const uint16_t*)d_in[1];
  const uint16_t* vc = (const uint16_t*)d_in[2];
  const uint16_t* wq = (const uint16_t*)d_in[3];
  const uint16_t* wo = (const uint16_t*)d_in[4];
  uint16_t* q = (uint16_t*)d_ws;
  float* po = (float*)((char*)d_ws + (1 << 20));
  float* pml = (float*)((char*)d_ws + (4 << 20));
  k_qproj<<<256, 512, 0, stream>>>(x, wq, q);
  k_attn<<<256, 512, 0, stream>>>(q, kc, vc, po, pml);
  k_out<<<256, 512, 0, stream>>>(po, pml, wo, (uint16_t*)d_out);
}
```

```cpp
#include <hip/hip_runtime.h>
#include <hip/hip_bf16.h>
#include <stdint.h>

typedef unsigned v4u __attribute__((ext_vector_type(4)));
typedef float f32x4 __attribute__((ext_vector_type(4)));
typedef short bf16x8 __attribute__((ext_vector_type(8)));

constexpr int B = 8, SQ = 4, D = 512, HQ = 32, HKV = 8, SK = 2048, DH = 128;
constexpr int NSPLIT = 4;

__device__ __forceinline__ f32x4 mfma(v4u a, v4u b, f32x4 c) {
  return __builtin_amdgcn_mfma_f32_16x16x32_bf16(__builtin_bit_cast(bf16x8, a), __builtin_bit_cast(bf16x8, b), c, 0, 0, 0);
}
__device__ __forceinline__ unsigned cvtpk(float lo, float hi) {
  unsigned r; asm volatile("v_cvt_pk_bf16_f32 %0, %1, %2" : "=v"(r) : "v"(lo), "v"(hi)); return r;
}
__device__ __forceinline__ void transpose8(const v4u (&in)[8], v4u (&out)[8]) {
#pragma unroll
  for (int w = 0; w < 4; ++w) {
#pragma unroll
    for (int u = 0; u < 4; ++u) {
      out[2 * w][u] = __builtin_amdgcn_perm(in[2 * u + 1][w], in[2 * u][w], 0x05040100u);
      out[2 * w + 1][u] = __builtin_amdgcn_perm(in[2 * u + 1][w], in[2 * u][w], 0x07060302u);
    }
  }
}

__global__ void __launch_bounds__(512) k_qproj(const uint16_t* __restrict__ x, const uint16_t* __restrict__ wq, uint16_t* __restrict__ q) {
  __shared__ float red[8][4][128];
  const int h = blockIdx.x & 31, b = blockIdx.x >> 5;
  const int tid = threadIdx.x, lane = tid & 63, w = tid >> 6, g = lane >> 4, c = lane & 15;
  f32x4 acc[8];
#pragma unroll
  for (int j = 0; j < 8; ++j) acc[j] = f32x4{0.f, 0.f, 0.f, 0.f};
#pragma unroll
  for (int ks = 0; ks < 2; ++ks) {
    const int k0 = 64 * w + 32 * ks;
    v4u in[8], fr[8];
#pragma unroll
    for (int i = 0; i < 8; ++i) in[i] = *(const v4u*)(wq + (size_t)(k0 + 8 * g + i) * (HQ * DH) + h * DH + 8 * c);
    v4u xf = *(const v4u*)(x + (size_t)(b * SQ + (c & 3)) * D + k0 + 8 * g);
    transpose8(in, fr);
#pragma unroll
    for (int j = 0; j < 8; ++j) acc[j] = mfma(xf, fr[j], acc[j]);
  }
  if (g == 0) {
#pragma unroll
    for (int j = 0; j < 8; ++j)
#pragma unroll
      for (int r = 0; r < 4; ++r) red[w][r][8 * c + j] = acc[j][r];
  }
  __syncthreads();
  {
    const int r = tid >> 7, n = tid & 127;
    float s = 0.f;
#pragma unroll
    for (int ww = 0; ww < 8; ++ww) s += red[ww][r][n];
    q[((size_t)(b * HQ + h) * SQ + r) * DH + n] = (uint16_t)(cvtpk(s, 0.f) & 0xffffu);
  }
}

__global__ void __launch_bounds__(512) k_attn(const uint16_t* __restrict__ q, const uint16_t* __restrict__ kc, const uint16_t* __restrict__ vc,
                                               float* __restrict__ po, float* __restrict__ pml) {
  __shared__ __attribute__((aligned(16))) float red[8][16][132];
  __shared__ float sml[8][16][2];
  const int kvh = blockIdx.x & 7, s = (blockIdx.x >> 3) & 3, b = blockIdx.x >> 5;
  const int tid = threadIdx.x, lane = tid & 63, w = tid >> 6, g = lane >> 4, c = lane & 15;
  const size_t base = ((size_t)(b * HKV + kvh) * SK + s * 512 + w * 64) * DH;
  const uint16_t* kp = kc + base;
  const uint16_t* vp = vc + base;
  v4u kf[4][4], vf[2][8];
#pragma unroll
  for (int t = 0; t < 4; ++t)
#pragma unroll
    for (int st = 0; st < 4; ++st) kf[t][st] = __builtin_nontemporal_load((const v4u*)(kp + (size_t)(16 * t + c) * DH + 32 * st + 8 * g));
#pragma unroll
  for (int bl = 0; bl < 2; ++bl)
#pragma unroll
    for (int i = 0; i < 8; ++i) vf[bl][i] = __builtin_nontemporal_load((const v4u*)(vp + (size_t)(32 * bl + (i >> 2) * 16 + 4 * g + (i & 3)) * DH + 8 * c));
  v4u qf[4];
#pragma unroll
  for (int st = 0; st < 4; ++st) qf[st] = *(const v4u*)(q + ((size_t)(b * HQ + kvh * 4) * SQ + c) * DH + 32 * st + 8 * g);
  f32x4 sc[4];
#pragma unroll
  for (int t = 0; t < 4; ++t) {
    sc[t] = f32x4{0.f, 0.f, 0.f, 0.f};
#pragma unroll
    for (int st = 0; st < 4; ++st) sc[t] = mfma(kf[t][st], qf[st], sc[t]);
  }
  constexpr float C2 = 0.088388347648318440f * 1.4426950408889634f;
  float m = -INFINITY;
#pragma unroll
  for (int t = 0; t < 4; ++t)
#pragma unroll
    for (int r = 0; r < 4; ++r) m = fmaxf(m, sc[t][r]);
  m = fmaxf(m, __shfl_xor(m, 16));
  m = fmaxf(m, __shfl_xor(m, 32));
  const float ms = m * C2;
  float l = 0.f;
#pragma unroll
  for (int t = 0; t < 4; ++t)
#pragma unroll
    for (int r = 0; r < 4; ++r) { sc[t][r] = __builtin_amdgcn_exp2f(sc[t][r] * C2 - ms); l += sc[t][r]; }
  l += __shfl_xor(l, 16);
  l += __shfl_xor(l, 32);
  f32x4 acc[8];
#pragma unroll
  for (int j = 0; j < 8; ++j) acc[j] = f32x4{0.f, 0.f, 0.f, 0.f};
#pragma unroll
  for (int bl = 0; bl < 2; ++bl) {
    v4u pf = {cvtpk(sc[2 * bl][0], sc[2 * bl][1]), cvtpk(sc[2 * bl][2], sc[2 * bl][3]), cvtpk(sc[2 * bl + 1][0], sc[2 * bl + 1][1]), cvtpk(sc[2 * bl + 1][2], sc[2 * bl + 1][3])};
    v4u fr[8];
    transpose8(vf[bl], fr);
#pragma unroll
    for (int j = 0; j < 8; ++j) acc[j] = mfma(fr[j], pf, acc[j]);
  }
#pragma unroll
  for (int r = 0; r < 4; ++r) {
    *(f32x4*)&red[w][c][32 * g + 8 * r] = f32x4{acc[0][r], acc[1][r], acc[2][r], acc[3][r]};
    *(f32x4*)&red[w][c][32 * g + 8 * r + 4] = f32x4{acc[4][r], acc[5][r], acc[6][r], acc[7][r]};
  }
  if (g == 0) { sml[w][c][0] = ms; sml[w][c][1] = l; }
  __syncthreads();
  {
    const int row = tid >> 5, d0 = (tid & 31) * 4;
    float M = -INFINITY;
#pragma unroll
    for (int ww = 0; ww < 8; ++ww) M = fmaxf(M, sml[ww][row][0]);
    f32x4 o = {0.f, 0.f, 0.f, 0.f};
    float L = 0.f;
#pragma unroll
    for (int ww = 0; ww < 8; ++ww) {
      const float f = __builtin_amdgcn_exp2f(sml[ww][row][0] - M);
      L += f * sml[ww][row][1];
      o += f * *(const f32x4*)&red[ww][row][d0];
    }
    const size_t slot = ((size_t)(b * HKV + kvh) * 16 + row) * NSPLIT + s;
    *(f32x4*)(po + slot * DH + d0) = o;
    if ((tid & 31) == 0) { pml[slot * 2] = M; pml[slot * 2 + 1] = L; }
  }
}

__global__ void __launch_bounds__(512) k_out(const float* __restrict__ po, const float* __restrict__ pml, const uint16_t* __restrict__ wo, uint16_t* __restrict__ out) {
  __shared__ float o_s[4][4096];
  __shared__ float red[32][4][16];
  const int cb = blockIdx.x & 31, b = blockIdx.x >> 5;
  const int tid = threadIdx.x;
  for (int idx = tid; idx < 128 * 32; idx += 512) {
    const int row = idx >> 5, d0 = (idx & 31) * 4;
    const size_t slot = ((size_t)b * 128 + row) * NSPLIT;
    float M = -INFINITY;
#pragma unroll
    for (int s = 0; s < NSPLIT; ++s) M = fmaxf(M, pml[(slot + s) * 2]);
    float L = 0.f;
    f32x4 o = {0.f, 0.f, 0.f, 0.f};
#pragma unroll
    for (int s = 0; s < NSPLIT; ++s) {
      const float f = __builtin_amdgcn_exp2f(pml[(slot + s) * 2] - M);
      L += f * pml[(slot + s) * 2 + 1];
      o += f * *(const f32x4*)(po + (slot + s) * DH + d0);
    }
    const float inv = 1.f / L;
    const int h = row >> 2, pos = row & 3;
#pragma unroll
    for (int e = 0; e < 4; ++e) {
      const unsigned r = cvtpk(o[e] * inv, 0.f);
      o_s[pos][h * 128 + d0 + e] = __builtin_bit_cast(float, r << 16);
    }
  }
  __syncthreads();
  const int kg = tid >> 4, col = tid & 15;
  float a0 = 0.f, a1 = 0.f, a2 = 0.f, a3 = 0.f;
  for (int k = kg * 128; k < kg * 128 + 128; ++k) {
    const float wv = __builtin_bit_cast(float, (unsigned)wo[(size_t)k * D + cb * 16 + col] << 16);
    a0 += o_s[0][k] * wv; a1 += o_s[1][k] * wv; a2 += o_s[2][k] * wv; a3 += o_s[3][k] * wv;
  }
  red[kg][0][col] = a0; red[kg][1][col] = a1; red[kg][2][col] = a2; red[kg][3][col] = a3;
  __syncthreads();
  if (tid < 64) {
    const int pos = tid >> 4, cc = tid & 15;
    float s = 0.f;
    for (int k = 0; k < 32; ++k) s += red[k][pos][cc];
    out[(size_t)(b * SQ + pos) * D + cb * 16 + cc] = (uint16_t)(cvtpk(s, 0.f) & 0xffffu);
  }
}


__device__ __forceinline__ unsigned long long rt() {
  unsigned long long t;
  __builtin_amdgcn_sched_barrier(0);
  asm volatile("s_memrealtime %0\n\ts_waitcnt lgkmcnt(0)" : "=s"(t) :: "memory");
  __builtin_amdgcn_sched_barrier(0);
  return t;
}
template <bool NT> __device__ __forceinline__ v4u ldx(const void* p) { if (NT) return __builtin_nontemporal_load((const v4u*)p); return *(const v4u*)p; }
template <bool NT>
__global__ void __launch_bounds__(512) k_stream(const uint16_t* __restrict__ kc, const uint16_t* __restrict__ vc, unsigned* __restrict__ sink, unsigned long long* __restrict__ stamps) {
  const unsigned long long t0 = rt();
  const int tid = threadIdx.x, lane = tid & 63, w = __builtin_amdgcn_readfirstlane(tid >> 6), g = lane >> 4, c = lane & 15;
  const int kvh = blockIdx.x & 7, rest = blockIdx.x >> 3, s = rest & 3, b = rest >> 2;
  const size_t base = ((size_t)(b * 8 + kvh) * 2048 + s * 512) * 128;
  const uint16_t* kp = kc + base + w * 2048 + g * 128 + 8 * c;
  const uint16_t* vp = vc + base + w * 2048 + g * 128 + 8 * c;
  v4u kf[16], vf[16];
#pragma unroll
  for (int i = 0; i < 16; ++i) kf[i] = ldx<NT>(kp + (i >> 2) * (8 * 2048) + (i & 3) * 512);
#pragma unroll
  for (int i = 0; i < 16; ++i) vf[i] = ldx<NT>(vp + (i >> 2) * (8 * 2048) + (i & 3) * 512);
  __builtin_amdgcn_sched_barrier(0);
  v4u a = {0, 0, 0, 0};
#pragma unroll
  for (int i = 0; i < 16; ++i) a ^= kf[i] ^ vf[i];
  unsigned r = a[0] ^ a[1] ^ a[2] ^ a[3];
  asm volatile("" : "+v"(r));
  const unsigned long long t1 = rt();
  if (r == 0x12345678u) sink[tid] = r;
  if (lane == 0) { stamps[(blockIdx.x * 8 + w) * 2] = t0; stamps[(blockIdx.x * 8 + w) * 2 + 1] = t1; }
}
__global__ void k_delay(const unsigned long long* __restrict__ stamps, int mult) {
  unsigned long long mn = ~0ull, mx = 0;
  for (int i = threadIdx.x; i < 2048; i += 64) { mn = min(mn, stamps[2 * i]); mx = max(mx, stamps[2 * i + 1]); }
  for (int o = 32; o; o >>= 1) { mn = min(mn, (unsigned long long)__shfl_xor((long long)mn, o)); mx = max(mx, (unsigned long long)__shfl_xor((long long)mx, o)); }
  unsigned long long span = mx - mn; if (span > 10000) span = 10000;
  const unsigned long long t0 = rt();
  while (rt() - t0 < span * (unsigned long long)mult) {}
}
extern "C" void kernel_launch(void* const* d_in, const int* in_sizes, int n_in, void* d_out, int out_size, void* d_ws, size_t ws_size,
                              hipStream_t stream) {
  const uint16_t* x = (const uint16_t*)d_in[0];
  const uint16_t* kc = (const uint16_t*)d_in[1];
  const uint16_t* vc = (const uint16_t*)d_in[2];
  const uint16_t* wq = (const uint16_t*)d_in[3];
  const uint16_t* wo = (const uint16_t*)d_in[4];
  uint16_t* q = (uint16_t*)d_ws;
  float* po = (float*)((char*)d_ws + (1 << 20));
  float* pml = (float*)((char*)d_ws + (4 << 20));
  k_stream<false><<<256, 512, 0, stream>>>(kc, vc, (unsigned*)((char*)d_ws + (8 << 20)), (unsigned long long*)((char*)d_ws + (9 << 20)));
  k_delay<<<1, 64, 0, stream>>>((const unsigned long long*)((char*)d_ws + (9 << 20)), 0);
  k_qproj<<<256, 512, 0, stream>>>(x, wq, q);
  k_attn<<<256, 512, 0, stream>>>(q, kc, vc, po, pml);
  k_out<<<256, 512, 0, stream>>>(po, pml, wo, (uint16_t*)d_out);
}
```

```cpp
#include <hip/hip_runtime.h>
#include <stdint.h>

#define LAS __attribute__((address_space(3)))
#define GAS __attribute__((address_space(1)))
typedef unsigned v4u __attribute__((ext_vector_type(4)));
typedef unsigned v2u __attribute__((ext_vector_type(2)));
typedef float f32x4 __attribute__((ext_vector_type(4)));
typedef short bf16x8 __attribute__((ext_vector_type(8)));

#define WO_EARLY 0
#define WNT 1

constexpr int WS_EPOCH = 0;
constexpr int WS_Q = 4096;
constexpr int WS_STAT = WS_Q + 64 * 8192;
constexpr int WS_O = WS_STAT + 64 * 1024;
constexpr int WS_Y = WS_O + 256 * 8192;
constexpr int WS_END = WS_Y + 256 * 8192;
constexpr int WS_STAMP = WS_END;
constexpr int L_K = 0;
constexpr int L_QP = 131072;
constexpr int L_QI = L_QP + 16384;
constexpr int L_AI = L_QI + 4352;
constexpr int L_MAX = L_AI + 4352;
constexpr int L_SUM = L_MAX + 256;
constexpr int L_SCALE = L_SUM + 256;
constexpr int L_CNT = L_SCALE + 64;
constexpr int L_END = L_CNT + 64;
constexpr unsigned SPIN_CAP = 1u << 18;

#define SB() __builtin_amdgcn_sched_barrier(0)
#define CB() asm volatile("" ::: "memory")

__device__ __forceinline__ f32x4 mfma(v4u a, v4u b, f32x4 c) {
  return __builtin_amdgcn_mfma_f32_16x16x32_bf16(__builtin_bit_cast(bf16x8, a), __builtin_bit_cast(bf16x8, b), c, 0, 0, 0);
}
__device__ __forceinline__ unsigned cvtpk(float lo, float hi) {
  unsigned r; asm("v_cvt_pk_bf16_f32 %0, %1, %2" : "=v"(r) : "v"(lo), "v"(hi)); return r;
}
__device__ __forceinline__ void transpose8(const v4u* in, v4u* out) {
#pragma unroll
  for (int w = 0; w < 4; ++w) {
#pragma unroll
    for (int u = 0; u < 4; ++u) {
      out[2 * w][u] = __builtin_amdgcn_perm(in[2 * u + 1][w], in[2 * u][w], 0x05040100u);
      out[2 * w + 1][u] = __builtin_amdgcn_perm(in[2 * u + 1][w], in[2 * u][w], 0x07060302u);
    }
  }
}
__device__ __forceinline__ v4u ldnt(const void* p) { return __builtin_nontemporal_load((const GAS v4u*)p); }
__device__ __forceinline__ v4u ldw(const void* p) {
  return __builtin_nontemporal_load((const GAS v4u*)p);
}
__device__ __forceinline__ float xor16(float x, bool mx) {
  auto s = __builtin_amdgcn_permlane16_swap(__float_as_uint(x), __float_as_uint(x), false, false);
  return mx ? fmaxf(__uint_as_float(s[0]), __uint_as_float(s[1])) : __uint_as_float(s[0]) + __uint_as_float(s[1]);
}
__device__ __forceinline__ float xor32(float x, bool mx) {
  auto s = __builtin_amdgcn_permlane32_swap(__float_as_uint(x), __float_as_uint(x), false, false);
  return mx ? fmaxf(__uint_as_float(s[0]), __uint_as_float(s[1])) : __uint_as_float(s[0]) + __uint_as_float(s[1]);
}
__device__ __forceinline__ float bflo(unsigned u) { return __uint_as_float(u << 16); }
__device__ __forceinline__ float bfhi(unsigned u) { return __uint_as_float(u & 0xffff0000u); }

__device__ __forceinline__ void lds_signal(LAS unsigned* cnt, int lane) {
  asm volatile("s_waitcnt lgkmcnt(0)" ::: "memory");
  if (lane == 0) __hip_atomic_fetch_add(cnt, 1u, __ATOMIC_RELAXED, __HIP_MEMORY_SCOPE_WORKGROUP);
  CB();
}
template <int SLEEP>
__device__ __forceinline__ void lds_wait(LAS unsigned* cnt, unsigned target) {
  CB();
  _Pragma("nounroll") for (unsigned spins = 0; spins < (SPIN_CAP << 4); ++spins) {
    const unsigned v = __builtin_amdgcn_readfirstlane(__hip_atomic_load(cnt, __ATOMIC_RELAXED, __HIP_MEMORY_SCOPE_WORKGROUP));
    if (v >= target) break;
    if (SLEEP) __builtin_amdgcn_s_sleep(SLEEP);
  }
  CB();
}
#define RAWBAR() do { SB(); asm volatile("s_waitcnt lgkmcnt(0)" ::: "memory"); __builtin_amdgcn_s_barrier(); CB(); SB(); } while (0)

#define STAMP(i) do {} while (0)

__global__ void __launch_bounds__(512, 2) gqa_fused(const uint16_t* __restrict__ x, const uint16_t* __restrict__ kc, const uint16_t* __restrict__ vc,
                                                     const uint16_t* __restrict__ wq, const uint16_t* __restrict__ wo, uint16_t* __restrict__ out, unsigned char* ws) {
  extern __shared__ __attribute__((aligned(16))) unsigned char smem[];
  LAS unsigned char* L = (LAS unsigned char*)smem;
  LAS unsigned* cnt = (LAS unsigned*)(L + L_CNT);
  STAMP(0);
  const int tid = threadIdx.x, lane = tid & 63, w = __builtin_amdgcn_readfirstlane(tid >> 6), g = lane >> 4, c = lane & 15;
  const int bid = blockIdx.x, kvh = bid & 7, s = (bid >> 3) & 3, b = bid >> 5;
  const __amdgpu_buffer_rsrc_t rs = __builtin_amdgcn_make_buffer_rsrc((void*)ws, 0, WS_END, 0x00020000);
  const size_t kvbase = ((size_t)(b * 8 + kvh) * 2048 + s * 512) * 128;
  const int h = kvh * 4 + s;
  constexpr float C2 = 0.088388347648318440f * 1.4426950408889634f;
  unsigned tag;
  v4u wf[32];

  if (w < 4) {
    const int b0 = b & ~1, hh = (b & 1) * 4 + s;
    const unsigned e0 = __builtin_amdgcn_raw_buffer_load_b32(rs, WS_EPOCH + b0 * 256, 0, 16);
    const unsigned e1 = __builtin_amdgcn_raw_buffer_load_b32(rs, WS_EPOCH + b0 * 256 + 256, 0, 16);
    v4u xf[2], wr[16];
#pragma unroll
    for (int u = 0; u < 2; ++u) xf[u] = *(const GAS v4u*)(x + (size_t)(b0 * 4 + (c & 7)) * 512 + 128 * w + 64 * u + 32 * (c >> 3) + 8 * g);
    {
      const uint16_t* p = wq + (size_t)(128 * w + 32 * (c >> 3) + 8 * g) * 4096 + kvh * 512 + hh * 64 + 8 * (c & 7);
#pragma unroll
      for (int i = 0; i < 16; ++i) wr[i] = ldw(p + (size_t)(64 * (i >> 3) + (i & 7)) * 4096);
    }
    SB();
    if (tid < 16) cnt[tid] = 0u;
    RAWBAR();
    f32x4 acc[8];
#pragma unroll
    for (int j = 0; j < 8; ++j) acc[j] = f32x4{0.f, 0.f, 0.f, 0.f};
#pragma unroll
    for (int u = 0; u < 2; ++u) {
      v4u fr[8];
      transpose8(wr + 8 * u, fr);
#pragma unroll
      for (int j = 0; j < 8; ++j) acc[j] = mfma(xf[u], fr[j], acc[j]);
    }
    STAMP(1);
    if ((g >> 1) == (c >> 3)) {
      LAS unsigned char* p = L + L_QP + ((w * 2 + (g >> 1)) * 8 + 4 * (g & 1)) * 256 + (c & 7) * 32;
#pragma unroll
      for (int r = 0; r < 4; ++r) {
        *(LAS f32x4*)(p + r * 256) = f32x4{acc[0][r], acc[1][r], acc[2][r], acc[3][r]};
        *(LAS f32x4*)(p + r * 256 + 16) = f32x4{acc[4][r], acc[5][r], acc[6][r], acc[7][r]};
      }
    }
    lds_signal(cnt + 0, lane);
    const unsigned tag0 = __builtin_amdgcn_readfirstlane(e0) + 1u, tag1 = __builtin_amdgcn_readfirstlane(e1) + 1u;
    tag = (b & 1) ? tag1 : tag0;
    lds_wait<0>(cnt + 0, 4u);
    if (lane < 32) {
      const int t = w * 32 + lane, r = t >> 4, nq = t & 15;
      f32x4 sum = {0.f, 0.f, 0.f, 0.f};
#pragma unroll
      for (int p = 0; p < 8; ++p) sum += *(const LAS f32x4*)(L + L_QP + (p * 8 + r) * 256 + nq * 16);
      const unsigned tg = (w >> 1) ? tag1 : tag0;
      const v4u unit = {cvtpk(sum[0], sum[1]), tg, cvtpk(sum[2], sum[3]), tg};
      const int row = (hh >> 1) * 4 + (r & 3), dq = (hh & 1) * 16 + nq;
      __builtin_amdgcn_raw_buffer_store_b128(unit, rs, WS_Q + ((b0 + (w >> 1)) * 8 + kvh) * 8192 + (row * 32 + dq) * 16, 0, 16);
    }
    STAMP(2);
    const uint16_t* wop = wo + (size_t)(h * 128 + 8 * g) * 512 + 128 * w + 8 * c;
    {
      const unsigned char* kp = (const unsigned char*)(kc + kvbase) + w * 4096 + g * 256;
#pragma unroll
      for (int t = 0; t < 8; ++t) {
#pragma unroll
        for (int i = 0; i < 4; ++i) {
          __builtin_amdgcn_global_load_lds((const GAS void*)(kp + t * 16384 + i * 1024 + ((c ^ (4 * g + i)) << 4)),
                                           (LAS void*)(L + L_K + (w + 4 * t) * 4096 + i * 1024), 16, 0, 2);
        }
#pragma unroll
        for (int i = t * WO_EARLY / 8; i < (t + 1) * WO_EARLY / 8; ++i) wf[i] = ldw(wop + (size_t)(32 * (i >> 3) + (i & 7)) * 512);
        SB();
      }
    }
    STAMP(3);
    {
      const int off = WS_Q + (b * 8 + kvh) * 8192 + tid * 16;
      v4u u0, u1;
      _Pragma("nounroll") for (unsigned spins = 0; spins < SPIN_CAP; ++spins) {
        CB();
        u0 = __builtin_amdgcn_raw_buffer_load_b128(rs, off, 0, 16);
        u1 = __builtin_amdgcn_raw_buffer_load_b128(rs, off + 4096, 0, 16);
        const bool ok = u0[1] == tag && u0[3] == tag && u1[1] == tag && u1[3] == tag;
        if (__all(ok)) break;
        __builtin_amdgcn_s_sleep(2);
      }
      *(LAS v2u*)(L + L_QI + (tid >> 5) * 272 + (tid & 31) * 8) = v2u{u0[0], u0[2]};
      *(LAS v2u*)(L + L_QI + ((tid >> 5) + 8) * 272 + (tid & 31) * 8) = v2u{u1[0], u1[2]};
    }
    asm volatile("s_waitcnt vmcnt(0)" ::: "memory");
    STAMP(4);
    lds_signal(cnt + 1, lane);
    lds_wait<0>(cnt + 1, 4u);
    v4u qf[4];
#pragma unroll
    for (int stp = 0; stp < 4; ++stp) qf[stp] = *(const LAS v4u*)(L + L_QI + c * 272 + stp * 64 + g * 16);
    f32x4 sc[8];
    {
      const int krow = 4 * (c & 3) + (c >> 2);
#pragma unroll
      for (int t = 0; t < 8; ++t) {
        sc[t] = f32x4{0.f, 0.f, 0.f, 0.f};
#pragma unroll
        for (int stp = 0; stp < 4; ++stp) {
          const v4u kf = *(const LAS v4u*)(L + L_K + (w + 4 * t) * 4096 + krow * 256 + (((4 * stp + g) ^ c) << 4));
          sc[t] = mfma(kf, qf[stp], sc[t]);
        }
      }
    }
    float m = sc[0][0];
#pragma unroll
    for (int t = 0; t < 8; ++t)
#pragma unroll
      for (int r = 0; r < 4; ++r) m = fmaxf(m, sc[t][r]);
    m = xor32(xor16(m, true), true);
    if (g == 0) *(LAS float*)(L + L_MAX + (w * 16 + c) * 4) = m;
    lds_signal(cnt + 2, lane);
    lds_wait<0>(cnt + 2, 4u);
#pragma unroll
    for (int ww = 0; ww < 4; ++ww) m = fmaxf(m, *(const LAS float*)(L + L_MAX + (ww * 16 + c) * 4));
    const float ms = m * C2;
    float l = 0.f;
#pragma unroll
    for (int t = 0; t < 8; ++t)
#pragma unroll
      for (int r = 0; r < 4; ++r) { sc[t][r] = __builtin_amdgcn_exp2f(fmaf(sc[t][r], C2, -ms)); l += sc[t][r]; }
#pragma unroll
    for (int jj = 0; jj < 4; ++jj) {
      const v4u pf = {cvtpk(sc[2 * jj][0], sc[2 * jj][1]), cvtpk(sc[2 * jj][2], sc[2 * jj][3]), cvtpk(sc[2 * jj + 1][0], sc[2 * jj + 1][1]), cvtpk(sc[2 * jj + 1][2], sc[2 * jj + 1][3])};
      *(LAS v4u*)(L + L_QP + (w * 4 + jj) * 1024 + lane * 16) = pf;
    }
    l = xor32(xor16(l, false), false);
    if (g == 0) *(LAS float*)(L + L_SUM + (w * 16 + c) * 4) = l;
    lds_signal(cnt + 4 + w, lane);
    lds_signal(cnt + 3, lane);
    STAMP(5);
    if (w == 0) {
      lds_wait<0>(cnt + 3, 4u);
      float L4 = 0.f;
#pragma unroll
      for (int ww = 0; ww < 4; ++ww) L4 += *(const LAS float*)(L + L_SUM + (ww * 16 + c) * 4);
      const int sbase = WS_STAT + (b * 8 + kvh) * 1024;
      if (g == 0) {
        const v4u unit = {__float_as_uint(ms), tag, __float_as_uint(L4), tag};
        __builtin_amdgcn_raw_buffer_store_b128(unit, rs, sbase + (s * 16 + c) * 16, 0, 16);
      }
    }
#pragma unroll
    for (int i = WO_EARLY; i < 32; ++i) wf[i] = ldw(wop + (size_t)(32 * (i >> 3) + (i & 7)) * 512);
    SB();
    STAMP(6);
    if (w == 0) {
      const int sbase = WS_STAT + (b * 8 + kvh) * 1024;
      v4u u;
      _Pragma("nounroll") for (unsigned spins = 0; spins < SPIN_CAP; ++spins) {
        CB();
        u = __builtin_amdgcn_raw_buffer_load_b128(rs, sbase + lane * 16, 0, 16);
        if (__all(u[1] == tag && u[3] == tag)) break;
        __builtin_amdgcn_s_sleep(2);
      }
      const float mo = __uint_as_float(u[0]), lo = __uint_as_float(u[2]);
      const float M = xor32(xor16(mo, true), true);
      const float Lt = xor32(xor16(__builtin_amdgcn_exp2f(mo - M) * lo, false), false);
      if (g == 0) *(LAS float*)(L + L_SCALE + c * 4) = __builtin_amdgcn_exp2f(ms - M) / Lt;
    }
#pragma unroll
    for (int u = 0; u < 4; ++u) {
      v4u fr[8];
      transpose8(wf + 8 * u, fr);
#pragma unroll
      for (int j = 0; j < 8; ++j) wf[8 * u + j] = fr[j];
    }
    STAMP(7);
  } else {
    const int a = w - 4;
    const unsigned e = __builtin_amdgcn_raw_buffer_load_b32(rs, WS_EPOCH + b * 256, 0, 16);
    const uint16_t* vp = vc + kvbase + a * 2048 + g * 128 + 8 * c;
#pragma unroll
    for (int i = 0; i < 4; ++i) wf[i] = ldnt(vp + (i >> 2) * 8192 + (i & 3) * 512);
    SB();
    RAWBAR();
#pragma unroll
    for (int i = 4; i < 32; ++i) wf[i] = ldnt(vp + (i >> 2) * 8192 + (i & 3) * 512);
    SB();
    STAMP(3);
    tag = __builtin_amdgcn_readfirstlane(e) + 1u;
    lds_wait<2>(cnt + 4 + a, 1u);
    STAMP(5);
    v4u pf[4];
#pragma unroll
    for (int jj = 0; jj < 4; ++jj) pf[jj] = *(const LAS v4u*)(L + L_QP + (a * 4 + jj) * 1024 + lane * 16);
    f32x4 acc[8];
#pragma unroll
    for (int j = 0; j < 8; ++j) acc[j] = f32x4{0.f, 0.f, 0.f, 0.f};
#pragma unroll
    for (int jj = 0; jj < 4; ++jj) {
      v4u fr[8];
      transpose8(wf + 8 * jj, fr);
#pragma unroll
      for (int j = 0; j < 8; ++j) acc[j] = mfma(fr[j], pf[jj], acc[j]);
    }
    STAMP(6);
    LAS unsigned char* p = L + L_K + (a + 4 * (c >> 3)) * 4096 + (c & 7) * 512;
#pragma unroll
    for (int r = 0; r < 4; ++r) {
      *(LAS f32x4*)(p + (((8 * g + 2 * r) ^ (c & 7)) << 4)) = f32x4{acc[0][r], acc[1][r], acc[2][r], acc[3][r]};
      *(LAS f32x4*)(p + (((8 * g + 2 * r + 1) ^ (c & 7)) << 4)) = f32x4{acc[4][r], acc[5][r], acc[6][r], acc[7][r]};
    }
    STAMP(7);
  }
  RAWBAR();
  STAMP(8);
  {
    const int row = tid >> 5, j = tid & 31;
    f32x4 o = {0.f, 0.f, 0.f, 0.f};
#pragma unroll
    for (int a = 0; a < 4; ++a) o += *(const LAS f32x4*)(L + L_K + (a + 4 * (row >> 3)) * 4096 + (row & 7) * 512 + ((j ^ (row & 7)) << 4));
    const float sf = *(const LAS float*)(L + L_SCALE + row * 4);
    const v4u unit = {cvtpk(o[0] * sf, o[1] * sf), tag, cvtpk(o[2] * sf, o[3] * sf), tag};
    __builtin_amdgcn_raw_buffer_store_b128(unit, rs, WS_O + ((b * 8 + kvh) * 4 + (row >> 2)) * 8192 + ((s * 4 + (row & 3)) * 32 + j) * 16, 0, 16);
    STAMP(9);
    const int off = WS_O + ((b * 8 + kvh) * 4 + s) * 8192 + tid * 16;
    v4u u;
    _Pragma("nounroll") for (unsigned spins = 0; spins < SPIN_CAP; ++spins) {
      CB();
      u = __builtin_amdgcn_raw_buffer_load_b128(rs, off, 0, 16);
      if (__all(u[1] == tag && u[3] == tag)) break;
    }
    *(LAS v2u*)(L + L_AI + (((tid >> 5) & 3) * 4 + (tid >> 7)) * 272 + j * 8) = v2u{u[0], u[2]};
  }
  STAMP(10);
  RAWBAR();
  STAMP(11);
  if (w < 4) {
    v4u af[4];
#pragma unroll
    for (int u = 0; u < 4; ++u) af[u] = *(const LAS v4u*)(L + L_AI + c * 272 + u * 64 + g * 16);
    f32x4 acc[8];
#pragma unroll
    for (int j = 0; j < 8; ++j) acc[j] = f32x4{0.f, 0.f, 0.f, 0.f};
#pragma unroll
    for (int u = 0; u < 4; ++u)
#pragma unroll
      for (int j = 0; j < 8; ++j) acc[j] = mfma(af[u], wf[8 * u + j], acc[j]);
    float y[8];
#pragma unroll
    for (int j = 0; j < 8; ++j) y[j] = (acc[j][0] + acc[j][1]) + (acc[j][2] + acc[j][3]);
    const int off = WS_Y + (b * 32 + 8 * w + (c >> 1)) * 8192 + ((h * 4 + g) * 4 + (c & 1) * 2) * 16;
    const v4u u0 = {cvtpk(y[0], y[1]), tag, cvtpk(y[2], y[3]), tag};
    const v4u u1 = {cvtpk(y[4], y[5]), tag, cvtpk(y[6], y[7]), tag};
    __builtin_amdgcn_raw_buffer_store_b128(u0, rs, off, 0, 16);
    __builtin_amdgcn_raw_buffer_store_b128(u1, rs, off + 16, 0, 16);
    STAMP(12);
  } else if (w == 4) {
    const int off = WS_Y + (b * 32 + h) * 8192 + g * 2048 + c * 16;
    v4u u[8];
    _Pragma("nounroll") for (unsigned spins = 0; spins < SPIN_CAP; ++spins) {
      CB();
      bool ok = true;
#pragma unroll
      for (int i = 0; i < 8; ++i) u[i] = __builtin_amdgcn_raw_buffer_load_b128(rs, off + i * 256, 0, 16);
#pragma unroll
      for (int i = 0; i < 8; ++i) ok = ok && u[i][1] == tag && u[i][3] == tag;
      if (__all(ok)) break;
    }
    STAMP(12);
    float y0 = 0.f, y1 = 0.f, y2 = 0.f, y3 = 0.f;
#pragma unroll
    for (int i = 0; i < 8; ++i) { y0 += bflo(u[i][0]); y1 += bfhi(u[i][0]); y2 += bflo(u[i][2]); y3 += bfhi(u[i][2]); }
    y0 = xor32(xor16(y0, false), false); y1 = xor32(xor16(y1, false), false);
    y2 = xor32(xor16(y2, false), false); y3 = xor32(xor16(y3, false), false);
    if (g == 0) *(GAS v2u*)(out + (size_t)(b * 4 + (c >> 2)) * 512 + 16 * h + 4 * (c & 3)) = v2u{cvtpk(y0, y1), cvtpk(y2, y3)};
    if (h == 0 && lane == 0) __builtin_amdgcn_raw_buffer_store_b32(tag, rs, WS_EPOCH + b * 256, 0, 16);
    STAMP(13);
  }
}

extern "C" void kernel_launch(void* const* d_in, const int* in_sizes, int n_in, void* d_out, int out_size, void* d_ws, size_t ws_size,
                              hipStream_t stream) {
  (void)hipFuncSetAttribute((const void*)gqa_fused, hipFuncAttributeMaxDynamicSharedMemorySize, L_END);
  gqa_fused<<<256, 512, L_END, stream>>>((const uint16_t*)d_in[0], (const uint16_t*)d_in[1], (const uint16_t*)d_in[2], (const uint16_t*)d_in[3],
                                         (const uint16_t*)d_in[4], (uint16_t*)d_out, (unsigned char*)d_ws);
}
```

```cpp
#define STAMPS 1
#include <hip/hip_runtime.h>
#include <stdint.h>

#define LAS __attribute__((address_space(3)))
#define GAS __attribute__((address_space(1)))
typedef unsigned v4u __attribute__((ext_vector_type(4)));
typedef unsigned v2u __attribute__((ext_vector_type(2)));
typedef float f32x4 __attribute__((ext_vector_type(4)));
typedef short bf16x8 __attribute__((ext_vector_type(8)));

#define WO_EARLY 16
#define WO_WIN 8
#define KSIG 8
#define WNT 1

constexpr int WS_EPOCH = 0;
constexpr int WS_Q = 4096;
constexpr int WS_STAT = WS_Q + 64 * 8192;
constexpr int WS_O = WS_STAT + 64 * 1024;
constexpr int WS_Y = WS_O + 256 * 8192;
constexpr int WS_END = WS_Y + 256 * 8192;
constexpr int WS_STAMP = WS_END;
constexpr int L_K = 0;
constexpr int L_QP = 131072;
constexpr int L_QI = L_QP + 16384;
constexpr int L_AI = L_QI + 4352;
constexpr int L_MAX = L_AI + 4352;
constexpr int L_SUM = L_MAX + 256;
constexpr int L_SCALE = L_SUM + 256;
constexpr int L_CNT = L_SCALE + 64;
constexpr int L_END = L_CNT + 64;
constexpr unsigned SPIN_CAP = 1u << 18;

#define SB() __builtin_amdgcn_sched_barrier(0)
#define CB() asm volatile("" ::: "memory")

__device__ __forceinline__ f32x4 mfma(v4u a, v4u b, f32x4 c) {
  return __builtin_amdgcn_mfma_f32_16x16x32_bf16(__builtin_bit_cast(bf16x8, a), __builtin_bit_cast(bf16x8, b), c, 0, 0, 0);
}
__device__ __forceinline__ unsigned cvtpk(float lo, float hi) {
  unsigned r; asm("v_cvt_pk_bf16_f32 %0, %1, %2" : "=v"(r) : "v"(lo), "v"(hi)); return r;
}
__device__ __forceinline__ void transpose8(const v4u* in, v4u* out) {
#pragma unroll
  for (int w = 0; w < 4; ++w) {
#pragma unroll
    for (int u = 0; u < 4; ++u) {
      out[2 * w][u] = __builtin_amdgcn_perm(in[2 * u + 1][w], in[2 * u][w], 0x05040100u);
      out[2 * w + 1][u] = __builtin_amdgcn_perm(in[2 * u + 1][w], in[2 * u][w], 0x07060302u);
    }
  }
}
__device__ __forceinline__ v4u ldnt(const void* p) { return __builtin_nontemporal_load((const GAS v4u*)p); }
__device__ __forceinline__ v4u ldw(const void* p) {
  return __builtin_nontemporal_load((const GAS v4u*)p);
}
__device__ __forceinline__ float xor16(float x, bool mx) {
  auto s = __builtin_amdgcn_permlane16_swap(__float_as_uint(x), __float_as_uint(x), false, false);
  return mx ? fmaxf(__uint_as_float(s[0]), __uint_as_float(s[1])) : __uint_as_float(s[0]) + __uint_as_float(s[1]);
}
__device__ __forceinline__ float xor32(float x, bool mx) {
  auto s = __builtin_amdgcn_permlane32_swap(__float_as_uint(x), __float_as_uint(x), false, false);
  return mx ? fmaxf(__uint_as_float(s[0]), __uint_as_float(s[1])) : __uint_as_float(s[0]) + __uint_as_float(s[1]);
}
__device__ __forceinline__ float bflo(unsigned u) { return __uint_as_float(u << 16); }
__device__ __forceinline__ float bfhi(unsigned u) { return __uint_as_float(u & 0xffff0000u); }

__device__ __forceinline__ void lds_signal(LAS unsigned* cnt, int lane) {
  asm volatile("s_waitcnt lgkmcnt(0)" ::: "memory");
  if (lane == 0) __hip_atomic_fetch_add(cnt, 1u, __ATOMIC_RELAXED, __HIP_MEMORY_SCOPE_WORKGROUP);
  CB();
}
template <int SLEEP>
__device__ __forceinline__ void lds_wait(LAS unsigned* cnt, unsigned target) {
  CB();
  _Pragma("nounroll") for (unsigned spins = 0; spins < (SPIN_CAP << 4); ++spins) {
    const unsigned v = __builtin_amdgcn_readfirstlane(__hip_atomic_load(cnt, __ATOMIC_RELAXED, __HIP_MEMORY_SCOPE_WORKGROUP));
    if (v >= target) break;
    if (SLEEP) __builtin_amdgcn_s_sleep(SLEEP);
  }
  CB();
}
#define RAWBAR() do { SB(); asm volatile("s_waitcnt lgkmcnt(0)" ::: "memory"); __builtin_amdgcn_s_barrier(); CB(); SB(); } while (0)

__device__ __forceinline__ void dma16(const void* gsrc, unsigned lds_dst) {
  unsigned keep;
  asm volatile("s_mov_b32 %0, m0\n\ts_mov_b32 m0, %2\n\ts_nop 0\n\tglobal_load_lds_dwordx4 %1, off nt\n\ts_mov_b32 m0, %0" : "=&s"(keep) : "v"(gsrc), "s"(lds_dst) : "memory");
}
template <int N> __device__ __forceinline__ void waitvm() { asm volatile("s_waitcnt vmcnt(%0)" :: "i"(N) : "memory"); }
template <int N, int STR, int SLEEP>
__device__ __forceinline__ void poll_units(__amdgpu_buffer_rsrc_t rs, int off, unsigned tag, v4u (&u)[N]) {
  int o[N];
#pragma unroll
  for (int i = 0; i < N; ++i) o[i] = off + i * STR;
  _Pragma("nounroll") for (unsigned spins = 0; spins < SPIN_CAP; ++spins) {
    CB();
    v4u t[N];
#pragma unroll
    for (int i = 0; i < N; ++i) t[i] = __builtin_amdgcn_raw_buffer_load_b128(rs, o[i], 0, 16);
    bool pend = false;
#pragma unroll
    for (int i = 0; i < N; ++i) {
      const bool was = o[i] >= 0;
      const bool ok = t[i][1] == tag && t[i][3] == tag;
      u[i][0] = was ? t[i][0] : u[i][0];
      u[i][2] = was ? t[i][2] : u[i][2];
      o[i] = (was && !ok) ? o[i] : (int)0x80000000;
      pend = pend || (was && !ok);
    }
    if (!__any(pend)) break;
    if (SLEEP) __builtin_amdgcn_s_sleep(SLEEP);
  }
}

#define STAMP(i) do { unsigned long long t_; SB(); asm volatile("s_memrealtime %0\n\ts_waitcnt lgkmcnt(0)" : "=s"(t_) :: "memory"); SB(); st[i] = t_; } while (0)

__global__ void __launch_bounds__(512, 2) gqa_fused(const uint16_t* __restrict__ x, const uint16_t* __restrict__ kc, const uint16_t* __restrict__ vc,
                                                     const uint16_t* __restrict__ wq, const uint16_t* __restrict__ wo, uint16_t* __restrict__ out, unsigned char* ws) {
  extern __shared__ __attribute__((aligned(16))) unsigned char smem[];
  LAS unsigned char* L = (LAS unsigned char*)smem;
  LAS unsigned* cnt = (LAS unsigned*)(L + L_CNT);
  unsigned long long st[16];
#pragma unroll
  for (int i = 0; i < 16; ++i) st[i] = 0;
  STAMP(0);
  const int tid = threadIdx.x, lane = tid & 63, w = __builtin_amdgcn_readfirstlane(tid >> 6), g = lane >> 4, c = lane & 15;
  const int bid = blockIdx.x, kvh = bid & 7, s = (bid >> 3) & 3, b = bid >> 5;
  const __amdgpu_buffer_rsrc_t rs = __builtin_amdgcn_make_buffer_rsrc((void*)ws, 0, WS_END, 0x00020000);
  const size_t kvbase = ((size_t)(b * 8 + kvh) * 2048 + s * 512) * 128;
  const int h = kvh * 4 + s;
  constexpr float C2 = 0.088388347648318440f * 1.4426950408889634f;
  unsigned tag;
  v4u wf[32];

  if (w < 4) {
    const int b0 = b & ~1, hh = (b & 1) * 4 + s;
    const unsigned e0 = __builtin_amdgcn_raw_buffer_load_b32(rs, WS_EPOCH + b0 * 256, 0, 16);
    const unsigned e1 = __builtin_amdgcn_raw_buffer_load_b32(rs, WS_EPOCH + b0 * 256 + 256, 0, 16);
    if (tid < 16) cnt[tid] = 0u;
    RAWBAR();
    v4u xf[2], wr[16];
#pragma unroll
    for (int u = 0; u < 2; ++u) xf[u] = *(const GAS v4u*)(x + (size_t)(b0 * 4 + (c & 7)) * 512 + 128 * w + 64 * u + 32 * (c >> 3) + 8 * g);
    {
      const uint16_t* p = wq + (size_t)(128 * w + 32 * (c >> 3) + 8 * g) * 4096 + kvh * 512 + hh * 64 + 8 * (c & 7);
#pragma unroll
      for (int i = 0; i < 16; ++i) wr[i] = ldw(p + (size_t)(64 * (i >> 3) + (i & 7)) * 4096);
    }
    SB();
    f32x4 acc[8];
#pragma unroll
    for (int j = 0; j < 8; ++j) acc[j] = f32x4{0.f, 0.f, 0.f, 0.f};
#pragma unroll
    for (int u = 0; u < 2; ++u) {
      v4u fr[8];
      transpose8(wr + 8 * u, fr);
#pragma unroll
      for (int j = 0; j < 8; ++j) acc[j] = mfma(xf[u], fr[j], acc[j]);
    }
    STAMP(1);
    if ((g >> 1) == (c >> 3)) {
      LAS unsigned char* p = L + L_QP + ((w * 2 + (g >> 1)) * 8 + 4 * (g & 1)) * 256 + (c & 7) * 32;
#pragma unroll
      for (int r = 0; r < 4; ++r) {
        *(LAS f32x4*)(p + r * 256) = f32x4{acc[0][r], acc[1][r], acc[2][r], acc[3][r]};
        *(LAS f32x4*)(p + r * 256 + 16) = f32x4{acc[4][r], acc[5][r], acc[6][r], acc[7][r]};
      }
    }
    lds_signal(cnt + 0, lane);
    const unsigned tag0 = __builtin_amdgcn_readfirstlane(e0) + 1u, tag1 = __builtin_amdgcn_readfirstlane(e1) + 1u;
    tag = (b & 1) ? tag1 : tag0;
    lds_wait<0>(cnt + 0, 4u);
    if (lane < 32) {
      const int t = w * 32 + lane, r = t >> 4, nq = t & 15;
      f32x4 sum = {0.f, 0.f, 0.f, 0.f};
#pragma unroll
      for (int p = 0; p < 8; ++p) sum += *(const LAS f32x4*)(L + L_QP + (p * 8 + r) * 256 + nq * 16);
      const unsigned tg = (w >> 1) ? tag1 : tag0;
      const v4u unit = {cvtpk(sum[0], sum[1]), tg, cvtpk(sum[2], sum[3]), tg};
      const int row = (hh >> 1) * 4 + (r & 3), dq = (hh & 1) * 16 + nq;
      __builtin_amdgcn_raw_buffer_store_b128(unit, rs, WS_Q + ((b0 + (w >> 1)) * 8 + kvh) * 8192 + (row * 32 + dq) * 16, 0, 16);
    }
    STAMP(2);
    const uint16_t* wop = wo + (size_t)(h * 128 + 8 * g) * 512 + 128 * w + 8 * c;
#pragma unroll
    for (int i = 0; i < WO_EARLY; ++i) { wf[i] = ldw(wop + (size_t)(32 * (i >> 3) + (i & 7)) * 512); SB(); waitvm<WO_WIN>(); SB(); }
    STAMP(3);
    {
      v4u u[2];
      poll_units<2, 4096, 2>(rs, WS_Q + (b * 8 + kvh) * 8192 + tid * 16, tag, u);
      *(LAS v2u*)(L + L_QI + (tid >> 5) * 272 + (tid & 31) * 8) = v2u{u[0][0], u[0][2]};
      *(LAS v2u*)(L + L_QI + ((tid >> 5) + 8) * 272 + (tid & 31) * 8) = v2u{u[1][0], u[1][2]};
    }
    STAMP(4);
    lds_signal(cnt + 1, lane);
    lds_wait<0>(cnt + 1, 4u);
    v4u qf[4];
#pragma unroll
    for (int stp = 0; stp < 4; ++stp) qf[stp] = *(const LAS v4u*)(L + L_QI + c * 272 + stp * 64 + g * 16);
    lds_wait<1>(cnt + 8 + w, 1u);
    STAMP(14);
    f32x4 sc[8];
    {
      const int krow = 4 * (c & 3) + (c >> 2);
#pragma unroll
      for (int t = 0; t < 8; ++t) {
        sc[t] = f32x4{0.f, 0.f, 0.f, 0.f};
#pragma unroll
        for (int stp = 0; stp < 4; ++stp) {
          const v4u kf = *(const LAS v4u*)(L + L_K + (w + 4 * t) * 4096 + krow * 256 + (((4 * stp + g) ^ c) << 4));
          sc[t] = mfma(kf, qf[stp], sc[t]);
        }
      }
    }
    float m = sc[0][0];
#pragma unroll
    for (int t = 0; t < 8; ++t)
#pragma unroll
      for (int r = 0; r < 4; ++r) m = fmaxf(m, sc[t][r]);
    m = xor32(xor16(m, true), true);
    if (g == 0) *(LAS float*)(L + L_MAX + (w * 16 + c) * 4) = m;
    lds_signal(cnt + 2, lane);
    lds_wait<0>(cnt + 2, 4u);
#pragma unroll
    for (int ww = 0; ww < 4; ++ww) m = fmaxf(m, *(const LAS float*)(L + L_MAX + (ww * 16 + c) * 4));
    const float ms = m * C2;
    float l = 0.f;
#pragma unroll
    for (int t = 0; t < 8; ++t)
#pragma unroll
      for (int r = 0; r < 4; ++r) { sc[t][r] = __builtin_amdgcn_exp2f(fmaf(sc[t][r], C2, -ms)); l += sc[t][r]; }
#pragma unroll
    for (int jj = 0; jj < 4; ++jj) {
      const v4u pf = {cvtpk(sc[2 * jj][0], sc[2 * jj][1]), cvtpk(sc[2 * jj][2], sc[2 * jj][3]), cvtpk(sc[2 * jj + 1][0], sc[2 * jj + 1][1]), cvtpk(sc[2 * jj + 1][2], sc[2 * jj + 1][3])};
      *(LAS v4u*)(L + L_QP + (w * 4 + jj) * 1024 + lane * 16) = pf;
    }
    l = xor32(xor16(l, false), false);
    if (g == 0) *(LAS float*)(L + L_SUM + (w * 16 + c) * 4) = l;
    lds_signal(cnt + 4 + w, lane);
    lds_signal(cnt + 3, lane);
    STAMP(5);
    if (w == 0) {
      lds_wait<0>(cnt + 3, 4u);
      float L4 = 0.f;
#pragma unroll
      for (int ww = 0; ww < 4; ++ww) L4 += *(const LAS float*)(L + L_SUM + (ww * 16 + c) * 4);
      const int sbase = WS_STAT + (b * 8 + kvh) * 1024;
      if (g == 0) {
        const v4u unit = {__float_as_uint(ms), tag, __float_as_uint(L4), tag};
        __builtin_amdgcn_raw_buffer_store_b128(unit, rs, sbase + (s * 16 + c) * 16, 0, 16);
      }
    }
#pragma unroll
    for (int i = WO_EARLY; i < 32; ++i) { wf[i] = ldw(wop + (size_t)(32 * (i >> 3) + (i & 7)) * 512); SB(); waitvm<WO_WIN>(); SB(); }
    STAMP(6);
    if (w == 0) {
      const int sbase = WS_STAT + (b * 8 + kvh) * 1024;
      v4u uu[1];
      poll_units<1, 0, 2>(rs, sbase + lane * 16, tag, uu);
      const v4u u = uu[0];
      const float mo = __uint_as_float(u[0]), lo = __uint_as_float(u[2]);
      const float M = xor32(xor16(mo, true), true);
      const float Lt = xor32(xor16(__builtin_amdgcn_exp2f(mo - M) * lo, false), false);
      if (g == 0) *(LAS float*)(L + L_SCALE + c * 4) = __builtin_amdgcn_exp2f(ms - M) / Lt;
    }
#pragma unroll
    for (int u = 0; u < 4; ++u) {
      v4u fr[8];
      transpose8(wf + 8 * u, fr);
#pragma unroll
      for (int j = 0; j < 8; ++j) wf[8 * u + j] = fr[j];
    }
    STAMP(7);
  } else {
    const int a = w - 4;
    const unsigned e = __builtin_amdgcn_raw_buffer_load_b32(rs, WS_EPOCH + b * 256, 0, 16);
    const uint16_t* vp = vc + kvbase + a * 2048 + g * 128 + 8 * c;
    {
      const unsigned char* kp = (const unsigned char*)(kc + kvbase) + a * 4096 + g * 256;
      const unsigned lb = (unsigned)(uintptr_t)(L + L_K) + a * 4096;
#pragma unroll
      for (int t = 0; t < 8; ++t) {
#pragma unroll
        for (int i = 0; i < 4; ++i) dma16(kp + t * 16384 + i * 1024 + ((c ^ (4 * g + i)) << 4), lb + t * 16384 + i * 1024);
        if (t == 0) RAWBAR();
      }
    }
    STAMP(2);
#pragma unroll
    for (int i = 0; i < KSIG; ++i) wf[i] = ldnt(vp + (i >> 2) * 8192 + (i & 3) * 512);
    SB();
    waitvm<KSIG>();
    lds_signal(cnt + 8 + a, lane);
    SB();
    STAMP(4);
#pragma unroll
    for (int i = KSIG; i < 32; ++i) wf[i] = ldnt(vp + (i >> 2) * 8192 + (i & 3) * 512);
    SB();
    STAMP(3);
    tag = __builtin_amdgcn_readfirstlane(e) + 1u;
    lds_wait<2>(cnt + 4 + a, 1u);
    STAMP(5);
    v4u pf[4];
#pragma unroll
    for (int jj = 0; jj < 4; ++jj) pf[jj] = *(const LAS v4u*)(L + L_QP + (a * 4 + jj) * 1024 + lane * 16);
    f32x4 acc[8];
#pragma unroll
    for (int j = 0; j < 8; ++j) acc[j] = f32x4{0.f, 0.f, 0.f, 0.f};
#pragma unroll
    for (int jj = 0; jj < 4; ++jj) {
      v4u fr[8];
      transpose8(wf + 8 * jj, fr);
#pragma unroll
      for (int j = 0; j < 8; ++j) acc[j] = mfma(fr[j], pf[jj], acc[j]);
    }
    STAMP(6);
    LAS unsigned char* p = L + L_K + (a + 4 * (c >> 3)) * 4096 + (c & 7) * 512;
#pragma unroll
    for (int r = 0; r < 4; ++r) {
      *(LAS f32x4*)(p + (((8 * g + 2 * r) ^ (c & 7)) << 4)) = f32x4{acc[0][r], acc[1][r], acc[2][r], acc[3][r]};
      *(LAS f32x4*)(p + (((8 * g + 2 * r + 1) ^ (c & 7)) << 4)) = f32x4{acc[4][r], acc[5][r], acc[6][r], acc[7][r]};
    }
    STAMP(7);
  }
  RAWBAR();
  STAMP(8);
  {
    const int row = tid >> 5, j = tid & 31;
    f32x4 o = {0.f, 0.f, 0.f, 0.f};
#pragma unroll
    for (int a = 0; a < 4; ++a) o += *(const LAS f32x4*)(L + L_K + (a + 4 * (row >> 3)) * 4096 + (row & 7) * 512 + ((j ^ (row & 7)) << 4));
    const float sf = *(const LAS float*)(L + L_SCALE + row * 4);
    const v4u unit = {cvtpk(o[0] * sf, o[1] * sf), tag, cvtpk(o[2] * sf, o[3] * sf), tag};
    __builtin_amdgcn_raw_buffer_store_b128(unit, rs, WS_O + ((b * 8 + kvh) * 4 + (row >> 2)) * 8192 + ((s * 4 + (row & 3)) * 32 + j) * 16, 0, 16);
    STAMP(9);
    const int off = WS_O + ((b * 8 + kvh) * 4 + s) * 8192 + tid * 16;
    v4u uu[1];
    poll_units<1, 0, 0>(rs, off, tag, uu);
    const v4u u = uu[0];
    *(LAS v2u*)(L + L_AI + (((tid >> 5) & 3) * 4 + (tid >> 7)) * 272 + j * 8) = v2u{u[0], u[2]};
  }
  STAMP(10);
  RAWBAR();
  STAMP(11);
  if (w < 4) {
    v4u af[4];
#pragma unroll
    for (int u = 0; u < 4; ++u) af[u] = *(const LAS v4u*)(L + L_AI + c * 272 + u * 64 + g * 16);
    f32x4 acc[8];
#pragma unroll
    for (int j = 0; j < 8; ++j) acc[j] = f32x4{0.f, 0.f, 0.f, 0.f};
#pragma unroll
    for (int u = 0; u < 4; ++u)
#pragma unroll
      for (int j = 0; j < 8; ++j) acc[j] = mfma(af[u], wf[8 * u + j], acc[j]);
    float y[8];
#pragma unroll
    for (int j = 0; j < 8; ++j) y[j] = (acc[j][0] + acc[j][1]) + (acc[j][2] + acc[j][3]);
    const int off = WS_Y + (b * 32 + 8 * w + (c >> 1)) * 8192 + ((h * 4 + g) * 4 + (c & 1) * 2) * 16;
    const v4u u0 = {cvtpk(y[0], y[1]), tag, cvtpk(y[2], y[3]), tag};
    const v4u u1 = {cvtpk(y[4], y[5]), tag, cvtpk(y[6], y[7]), tag};
    __builtin_amdgcn_raw_buffer_store_b128(u0, rs, off, 0, 16);
    __builtin_amdgcn_raw_buffer_store_b128(u1, rs, off + 16, 0, 16);
    STAMP(12);
  } else if (w == 4) {
    const int off = WS_Y + (b * 32 + h) * 8192 + g * 2048 + c * 16;
    v4u u[8];
    poll_units<8, 256, 0>(rs, off, tag, u);
    STAMP(12);
    float y0 = 0.f, y1 = 0.f, y2 = 0.f, y3 = 0.f;
#pragma unroll
    for (int i = 0; i < 8; ++i) { y0 += bflo(u[i][0]); y1 += bfhi(u[i][0]); y2 += bflo(u[i][2]); y3 += bfhi(u[i][2]); }
    y0 = xor32(xor16(y0, false), false); y1 = xor32(xor16(y1, false), false);
    y2 = xor32(xor16(y2, false), false); y3 = xor32(xor16(y3, false), false);
    if (g == 0) *(GAS v2u*)(out + (size_t)(b * 4 + (c >> 2)) * 512 + 16 * h + 4 * (c & 3)) = v2u{cvtpk(y0, y1), cvtpk(y2, y3)};
    if (h == 0 && lane == 0) __builtin_amdgcn_raw_buffer_store_b32(tag, rs, WS_EPOCH + b * 256, 0, 16);
    STAMP(13);
  }
  if (lane == 0) {
    unsigned long long* sp = (unsigned long long*)(ws + WS_STAMP) + (size_t)(bid * 8 + w) * 16;
#pragma unroll
    for (int i = 0; i < 16; ++i) sp[i] = st[i];
  }
}

__device__ __forceinline__ unsigned long long rt_() { unsigned long long t; asm volatile("s_memrealtime %0\n\ts_waitcnt lgkmcnt(0)" : "=s"(t) :: "memory"); return t; }
__device__ unsigned long long stat_(const unsigned long long* st, int role, int idx, int kind, unsigned long long base) {
  unsigned long long mn = ~0ull, mx = 0, sum = 0, n = 0;
  for (int i = threadIdx.x; i < 2048; i += 64) { const int w = i & 7; if (role != 2 && (w >> 2) != role) continue; const unsigned long long t = st[i * 16 + idx]; if (!t) continue; mn = min(mn, t); mx = max(mx, t); sum += t - base; n += 1; }
  for (int o = 32; o; o >>= 1) { mn = min(mn, (unsigned long long)__shfl_xor((long long)mn, o)); mx = max(mx, (unsigned long long)__shfl_xor((long long)mx, o)); sum += (unsigned long long)__shfl_xor((long long)sum, o); n += (unsigned long long)__shfl_xor((long long)n, o); }
  return kind == 0 ? mn : (kind == 1 ? mx : base + sum / n);
}
__global__ void k_delay(const unsigned long long* __restrict__ st) {
  const unsigned long long base = st[0] - 100000ull;
  unsigned long long span = stat_(st, 1, 13, 1, base) - stat_(st, 2, 0, 0, base); if (span > 10000) span = 10000;
  const unsigned long long t0 = rt_();
  while (rt_() - t0 < span * (unsigned long long)0) {}
}
extern "C" void kernel_launch(void* const* d_in, const int* in_sizes, int n_in, void* d_out, int out_size, void* d_ws, size_t ws_size,
                              hipStream_t stream) {
  (void)hipFuncSetAttribute((const void*)gqa_fused, hipFuncAttributeMaxDynamicSharedMemorySize, L_END);
  gqa_fused<<<256, 512, L_END, stream>>>((const uint16_t*)d_in[0], (const uint16_t*)d_in[1], (const uint16_t*)d_in[2], (const uint16_t*)d_in[3],
                                         (const uint16_t*)d_in[4], (uint16_t*)d_out, (unsigned char*)d_ws);
  k_delay<<<1, 64, 0, stream>>>((const unsigned long long*)((char*)d_ws + WS_STAMP));
}
```

```cpp
#define STAMPS 1
#include <hip/hip_runtime.h>
#include <stdint.h>

#define LAS __attribute__((address_space(3)))
#define GAS __attribute__((address_space(1)))
typedef unsigned v4u __attribute__((ext_vector_type(4)));
typedef unsigned v2u __attribute__((ext_vector_type(2)));
typedef float f32x4 __attribute__((ext_vector_type(4)));
typedef short bf16x8 __attribute__((ext_vector_type(8)));

#define WO_EARLY 16
#define WO_WIN 8
#define KSIG 8
#define WNT 1

constexpr int WS_EPOCH = 0;
constexpr int WS_Q = 4096;
constexpr int WS_STAT = WS_Q + 64 * 8192;
constexpr int WS_O = WS_STAT + 64 * 1024;
constexpr int WS_Y = WS_O + 256 * 8192;
constexpr int WS_END = WS_Y + 256 * 8192;
constexpr int WS_STAMP = WS_END;
constexpr int L_K = 0;
constexpr int L_QP = 131072;
constexpr int L_QI = L_QP + 16384;
constexpr int L_AI = L_QI + 4352;
constexpr int L_MAX = L_AI + 4352;
constexpr int L_SUM = L_MAX + 256;
constexpr int L_SCALE = L_SUM + 256;
constexpr int L_CNT = L_SCALE + 64;
constexpr int L_END = L_CNT + 64;
constexpr unsigned SPIN_CAP = 1u << 18;

#define SB() __builtin_amdgcn_sched_barrier(0)
#define CB() asm volatile("" ::: "memory")

__device__ __forceinline__ f32x4 mfma(v4u a, v4u b, f32x4 c) {
  return __builtin_amdgcn_mfma_f32_16x16x32_bf16(__builtin_bit_cast(bf16x8, a), __builtin_bit_cast(bf16x8, b), c, 0, 0, 0);
}
__device__ __forceinline__ unsigned cvtpk(float lo, float hi) {
  unsigned r; asm("v_cvt_pk_bf16_f32 %0, %1, %2" : "=v"(r) : "v"(lo), "v"(hi)); return r;
}
__device__ __forceinline__ void transpose8(const v4u* in, v4u* out) {
#pragma unroll
  for (int w = 0; w < 4; ++w) {
#pragma unroll
    for (int u = 0; u < 4; ++u) {
      out[2 * w][u] = __builtin_amdgcn_perm(in[2 * u + 1][w], in[2 * u][w], 0x05040100u);
      out[2 * w + 1][u] = __builtin_amdgcn_perm(in[2 * u + 1][w], in[2 * u][w], 0x07060302u);
    }
  }
}
__device__ __forceinline__ v4u ldnt(const void* p) { return __builtin_nontemporal_load((const GAS v4u*)p); }
__device__ __forceinline__ v4u ldw(const void* p) {
  return __builtin_nontemporal_load((const GAS v4u*)p);
}
__device__ __forceinline__ float xor16(float x, bool mx) {
  auto s = __builtin_amdgcn_permlane16_swap(__float_as_uint(x), __float_as_uint(x), false, false);
  return mx ? fmaxf(__uint_as_float(s[0]), __uint_as_float(s[1])) : __uint_as_float(s[0]) + __uint_as_float(s[1]);
}
__device__ __forceinline__ float xor32(float x, bool mx) {
  auto s = __builtin_amdgcn_permlane32_swap(__float_as_uint(x), __float_as_uint(x), false, false);
  return mx ? fmaxf(__uint_as_float(s[0]), __uint_as_float(s[1])) : __uint_as_float(s[0]) + __uint_as_float(s[1]);
}
__device__ __forceinline__ float bflo(unsigned u) { return __uint_as_float(u << 16); }
__device__ __forceinline__ float bfhi(unsigned u) { return __uint_as_float(u & 0xffff0000u); }

__device__ __forceinline__ void lds_signal(LAS unsigned* cnt, int lane) {
  asm volatile("s_waitcnt lgkmcnt(0)" ::: "memory");
  if (lane == 0) __hip_atomic_fetch_add(cnt, 1u, __ATOMIC_RELAXED, __HIP_MEMORY_SCOPE_WORKGROUP);
  CB();
}
template <int SLEEP>
__device__ __forceinline__ void lds_wait(LAS unsigned* cnt, unsigned target) {
  CB();
  _Pragma("nounroll") for (unsigned spins = 0; spins < (SPIN_CAP << 4); ++spins) {
    const unsigned v = __builtin_amdgcn_readfirstlane(__hip_atomic_load(cnt, __ATOMIC_RELAXED, __HIP_MEMORY_SCOPE_WORKGROUP));
    if (v >= target) break;
    if (SLEEP) __builtin_amdgcn_s_sleep(SLEEP);
  }
  CB();
}
#define RAWBAR() do { SB(); asm volatile("s_waitcnt lgkmcnt(0)" ::: "memory"); __builtin_amdgcn_s_barrier(); CB(); SB(); } while (0)

__device__ __forceinline__ void dma16(const void* gsrc, unsigned lds_dst) {
  unsigned keep;
  asm volatile("s_mov_b32 %0, m0\n\ts_mov_b32 m0, %2\n\ts_nop 0\n\tglobal_load_lds_dwordx4 %1, off nt\n\ts_mov_b32 m0, %0" : "=&s"(keep) : "v"(gsrc), "s"(lds_dst) : "memory");
}
template <int N> __device__ __forceinline__ void waitvm() { asm volatile("s_waitcnt vmcnt(%0)" :: "i"(N) : "memory"); }
template <int N, int STR, int SLEEP>
__device__ __forceinline__ void poll_units(__amdgpu_buffer_rsrc_t rs, int off, unsigned tag, v4u (&u)[N]) {
  int o[N];
#pragma unroll
  for (int i = 0; i < N; ++i) o[i] = off + i * STR;
  _Pragma("nounroll") for (unsigned spins = 0; spins < SPIN_CAP; ++spins) {
    CB();
    v4u t[N];
#pragma unroll
    for (int i = 0; i < N; ++i) t[i] = __builtin_amdgcn_raw_buffer_load_b128(rs, o[i], 0, 16);
    bool pend = false;
#pragma unroll
    for (int i = 0; i < N; ++i) {
      const bool was = o[i] >= 0;
      const bool ok = t[i][1] == tag && t[i][3] == tag;
      u[i][0] = was ? t[i][0] : u[i][0];
      u[i][2] = was ? t[i][2] : u[i][2];
      o[i] = (was && !ok) ? o[i] : (int)0x80000000;
      pend = pend || (was && !ok);
    }
    if (!__any(pend)) break;
    if (SLEEP) __builtin_amdgcn_s_sleep(SLEEP);
  }
}

#define STAMP(i) do { unsigned long long t_; SB(); asm volatile("s_memrealtime %0\n\ts_waitcnt lgkmcnt(0)" : "=s"(t_) :: "memory"); SB(); st[i] = t_; } while (0)

__global__ void __launch_bounds__(512, 2) gqa_fused(const uint16_t* __restrict__ x, const uint16_t* __restrict__ kc, const uint16_t* __restrict__ vc,
                                                     const uint16_t* __restrict__ wq, const uint16_t* __restrict__ wo, uint16_t* __restrict__ out, unsigned char* ws) {
  extern __shared__ __attribute__((aligned(16))) unsigned char smem[];
  LAS unsigned char* L = (LAS unsigned char*)smem;
  LAS unsigned* cnt = (LAS unsigned*)(L + L_CNT);
  unsigned long long st[16];
#pragma unroll
  for (int i = 0; i < 16; ++i) st[i] = 0;
  STAMP(0);
  const int tid = threadIdx.x, lane = tid & 63, w = __builtin_amdgcn_readfirstlane(tid >> 6), g = lane >> 4, c = lane & 15;
  const int bid = blockIdx.x, kvh = bid & 7, s = (bid >> 3) & 3, b = bid >> 5;
  const __amdgpu_buffer_rsrc_t rs = __builtin_amdgcn_make_buffer_rsrc((void*)ws, 0, WS_END, 0x00020000);
  const size_t kvbase = ((size_t)(b * 8 + kvh) * 2048 + s * 512) * 128;
  const int h = kvh * 4 + s;
  constexpr float C2 = 0.088388347648318440f * 1.4426950408889634f;
  unsigned tag;
  v4u wf[32];

  if (w < 4) {
    const int b0 = b & ~1, hh = (b & 1) * 4 + s;
    const unsigned e0 = __builtin_amdgcn_raw_buffer_load_b32(rs, WS_EPOCH + b0 * 256, 0, 16);
    const unsigned e1 = __builtin_amdgcn_raw_buffer_load_b32(rs, WS_EPOCH + b0 * 256 + 256, 0, 16);
    if (tid < 16) cnt[tid] = 0u;
    RAWBAR();
    v4u xf[2], wr[16];
#pragma unroll
    for (int u = 0; u < 2; ++u) xf[u] = *(const GAS v4u*)(x + (size_t)(b0 * 4 + (c & 7)) * 512 + 128 * w + 64 * u + 32 * (c >> 3) + 8 * g);
    {
      const uint16_t* p = wq + (size_t)(128 * w + 32 * (c >> 3) + 8 * g) * 4096 + kvh * 512 + hh * 64 + 8 * (c & 7);
#pragma unroll
      for (int i = 0; i < 16; ++i) wr[i] = ldw(p + (size_t)(64 * (i >> 3) + (i & 7)) * 4096);
    }
    SB();
    f32x4 acc[8];
#pragma unroll
    for (int j = 0; j < 8; ++j) acc[j] = f32x4{0.f, 0.f, 0.f, 0.f};
#pragma unroll
    for (int u = 0; u < 2; ++u) {
      v4u fr[8];
      transpose8(wr + 8 * u, fr);
#pragma unroll
      for (int j = 0; j < 8; ++j) acc[j] = mfma(xf[u], fr[j], acc[j]);
    }
    STAMP(1);
    if ((g >> 1) == (c >> 3)) {
      LAS unsigned char* p = L + L_QP + ((w * 2 + (g >> 1)) * 8 + 4 * (g & 1)) * 256 + (c & 7) * 32;
#pragma unroll
      for (int r = 0; r < 4; ++r) {
        *(LAS f32x4*)(p + r * 256) = f32x4{acc[0][r], acc[1][r], acc[2][r], acc[3][r]};
        *(LAS f32x4*)(p + r * 256 + 16) = f32x4{acc[4][r], acc[5][r], acc[6][r], acc[7][r]};
      }
    }
    lds_signal(cnt + 0, lane);
    const unsigned tag0 = __builtin_amdgcn_readfirstlane(e0) + 1u, tag1 = __builtin_amdgcn_readfirstlane(e1) + 1u;
    tag = (b & 1) ? tag1 : tag0;
    lds_wait<0>(cnt + 0, 4u);
    if (lane < 32) {
      const int t = w * 32 + lane, r = t >> 4, nq = t & 15;
      f32x4 sum = {0.f, 0.f, 0.f, 0.f};
#pragma unroll
      for (int p = 0; p < 8; ++p) sum += *(const LAS f32x4*)(L + L_QP + (p * 8 + r) * 256 + nq * 16);
      const unsigned tg = (w >> 1) ? tag1 : tag0;
      const v4u unit = {cvtpk(sum[0], sum[1]), tg, cvtpk(sum[2], sum[3]), tg};
      const int row = (hh >> 1) * 4 + (r & 3), dq = (hh & 1) * 16 + nq;
      __builtin_amdgcn_raw_buffer_store_b128(unit, rs, WS_Q + ((b0 + (w >> 1)) * 8 + kvh) * 8192 + (row * 32 + dq) * 16, 0, 16);
    }
    STAMP(2);
    const uint16_t* wop = wo + (size_t)(h * 128 + 8 * g) * 512 + 128 * w + 8 * c;
#pragma unroll
    for (int i = 0; i < WO_EARLY; ++i) { wf[i] = ldw(wop + (size_t)(32 * (i >> 3) + (i & 7)) * 512); SB(); waitvm<WO_WIN>(); SB(); }
    STAMP(3);
    {
      v4u u[2];
      poll_units<2, 4096, 2>(rs, WS_Q + (b * 8 + kvh) * 8192 + tid * 16, tag, u);
      *(LAS v2u*)(L + L_QI + (tid >> 5) * 272 + (tid & 31) * 8) = v2u{u[0][0], u[0][2]};
      *(LAS v2u*)(L + L_QI + ((tid >> 5) + 8) * 272 + (tid & 31) * 8) = v2u{u[1][0], u[1][2]};
    }
    STAMP(4);
    lds_signal(cnt + 1, lane);
    lds_wait<0>(cnt + 1, 4u);
    v4u qf[4];
#pragma unroll
    for (int stp = 0; stp < 4; ++stp) qf[stp] = *(const LAS v4u*)(L + L_QI + c * 272 + stp * 64 + g * 16);
    lds_wait<1>(cnt + 8 + w, 1u);
    STAMP(14);
    f32x4 sc[8];
    {
      const int krow = 4 * (c & 3) + (c >> 2);
#pragma unroll
      for (int t = 0; t < 8; ++t) {
        sc[t] = f32x4{0.f, 0.f, 0.f, 0.f};
#pragma unroll
        for (int stp = 0; stp < 4; ++stp) {
          const v4u kf = *(const LAS v4u*)(L + L_K + (w + 4 * t) * 4096 + krow * 256 + (((4 * stp + g) ^ c) << 4));
          sc[t] = mfma(kf, qf[stp], sc[t]);
        }
      }
    }
    float m = sc[0][0];
#pragma unroll
    for (int t = 0; t < 8; ++t)
#pragma unroll
      for (int r = 0; r < 4; ++r) m = fmaxf(m, sc[t][r]);
    m = xor32(xor16(m, true), true);
    if (g == 0) *(LAS float*)(L + L_MAX + (w * 16 + c) * 4) = m;
    lds_signal(cnt + 2, lane);
    lds_wait<0>(cnt + 2, 4u);
#pragma unroll
    for (int ww = 0; ww < 4; ++ww) m = fmaxf(m, *(const LAS float*)(L + L_MAX + (ww * 16 + c) * 4));
    const float ms = m * C2;
    float l = 0.f;
#pragma unroll
    for (int t = 0; t < 8; ++t)
#pragma unroll
      for (int r = 0; r < 4; ++r) { sc[t][r] = __builtin_amdgcn_exp2f(fmaf(sc[t][r], C2, -ms)); l += sc[t][r]; }
#pragma unroll
    for (int jj = 0; jj < 4; ++jj) {
      const v4u pf = {cvtpk(sc[2 * jj][0], sc[2 * jj][1]), cvtpk(sc[2 * jj][2], sc[2 * jj][3]), cvtpk(sc[2 * jj + 1][0], sc[2 * jj + 1][1]), cvtpk(sc[2 * jj + 1][2], sc[2 * jj + 1][3])};
      *(LAS v4u*)(L + L_QP + (w * 4 + jj) * 1024 + lane * 16) = pf;
    }
    l = xor32(xor16(l, false), false);
    if (g == 0) *(LAS float*)(L + L_SUM + (w * 16 + c) * 4) = l;
    lds_signal(cnt + 4 + w, lane);
    lds_signal(cnt + 3, lane);
    STAMP(5);
    if (w == 0) {
      lds_wait<0>(cnt + 3, 4u);
      float L4 = 0.f;
#pragma unroll
      for (int ww = 0; ww < 4; ++ww) L4 += *(const LAS float*)(L + L_SUM + (ww * 16 + c) * 4);
      const int sbase = WS_STAT + (b * 8 + kvh) * 1024;
      if (g == 0) {
        const v4u unit = {__float_as_uint(ms), tag, __float_as_uint(L4), tag};
        __builtin_amdgcn_raw_buffer_store_b128(unit, rs, sbase + (s * 16 + c) * 16, 0, 16);
      }
    }
#pragma unroll
    for (int i = WO_EARLY; i < 32; ++i) { wf[i] = ldw(wop + (size_t)(32 * (i >> 3) + (i & 7)) * 512); SB(); waitvm<WO_WIN>(); SB(); }
    STAMP(6);
    if (w == 0) {
      const int sbase = WS_STAT + (b * 8 + kvh) * 1024;
      v4u uu[1];
      poll_units<1, 0, 2>(rs, sbase + lane * 16, tag, uu);
      const v4u u = uu[0];
      const float mo = __uint_as_float(u[0]), lo = __uint_as_float(u[2]);
      const float M = xor32(xor16(mo, true), true);
      const float Lt = xor32(xor16(__builtin_amdgcn_exp2f(mo - M) * lo, false), false);
      if (g == 0) *(LAS float*)(L + L_SCALE + c * 4) = __builtin_amdgcn_exp2f(ms - M) / Lt;
    }
#pragma unroll
    for (int u = 0; u < 4; ++u) {
      v4u fr[8];
      transpose8(wf + 8 * u, fr);
#pragma unroll
      for (int j = 0; j < 8; ++j) wf[8 * u + j] = fr[j];
    }
    STAMP(7);
  } else {
    const int a = w - 4;
    const unsigned e = __builtin_amdgcn_raw_buffer_load_b32(rs, WS_EPOCH + b * 256, 0, 16);
    const uint16_t* vp = vc + kvbase + a * 2048 + g * 128 + 8 * c;
    {
      const unsigned char* kp = (const unsigned char*)(kc + kvbase) + a * 4096 + g * 256;
      const unsigned lb = (unsigned)(uintptr_t)(L + L_K) + a * 4096;
#pragma unroll
      for (int t = 0; t < 8; ++t) {
#pragma unroll
        for (int i = 0; i < 4; ++i) dma16(kp + t * 16384 + i * 1024 + ((c ^ (4 * g + i)) << 4), lb + t * 16384 + i * 1024);
        if (t == 0) RAWBAR();
      }
    }
    STAMP(2);
#pragma unroll
    for (int i = 0; i < KSIG; ++i) wf[i] = ldnt(vp + (i >> 2) * 8192 + (i & 3) * 512);
    SB();
    waitvm<KSIG>();
    lds_signal(cnt + 8 + a, lane);
    SB();
    STAMP(4);
#pragma unroll
    for (int i = KSIG; i < 32; ++i) wf[i] = ldnt(vp + (i >> 2) * 8192 + (i & 3) * 512);
    SB();
    STAMP(3);
    tag = __builtin_amdgcn_readfirstlane(e) + 1u;
    lds_wait<2>(cnt + 4 + a, 1u);
    STAMP(5);
    v4u pf[4];
#pragma unroll
    for (int jj = 0; jj < 4; ++jj) pf[jj] = *(const LAS v4u*)(L + L_QP + (a * 4 + jj) * 1024 + lane * 16);
    f32x4 acc[8];
#pragma unroll
    for (int j = 0; j < 8; ++j) acc[j] = f32x4{0.f, 0.f, 0.f, 0.f};
#pragma unroll
    for (int jj = 0; jj < 4; ++jj) {
      v4u fr[8];
      transpose8(wf + 8 * jj, fr);
#pragma unroll
      for (int j = 0; j < 8; ++j) acc[j] = mfma(fr[j], pf[jj], acc[j]);
    }
    STAMP(6);
    LAS unsigned char* p = L + L_K + (a + 4 * (c >> 3)) * 4096 + (c & 7) * 512;
#pragma unroll
    for (int r = 0; r < 4; ++r) {
      *(LAS f32x4*)(p + (((8 * g + 2 * r) ^ (c & 7)) << 4)) = f32x4{acc[0][r], acc[1][r], acc[2][r], acc[3][r]};
      *(LAS f32x4*)(p + (((8 * g + 2 * r + 1) ^ (c & 7)) << 4)) = f32x4{acc[4][r], acc[5][r], acc[6][r], acc[7][r]};
    }
    STAMP(7);
  }
  RAWBAR();
  STAMP(8);
  {
    const int row = tid >> 5, j = tid & 31;
    f32x4 o = {0.f, 0.f, 0.f, 0.f};
#pragma unroll
    for (int a = 0; a < 4; ++a) o += *(const LAS f32x4*)(L + L_K + (a + 4 * (row >> 3)) * 4096 + (row & 7) * 512 + ((j ^ (row & 7)) << 4));
    const float sf = *(const LAS float*)(L + L_SCALE + row * 4);
    const v4u unit = {cvtpk(o[0] * sf, o[1] * sf), tag, cvtpk(o[2] * sf, o[3] * sf), tag};
    __builtin_amdgcn_raw_buffer_store_b128(unit, rs, WS_O + ((b * 8 + kvh) * 4 + (row >> 2)) * 8192 + ((s * 4 + (row & 3)) * 32 + j) * 16, 0, 16);
    STAMP(9);
    const int off = WS_O + ((b * 8 + kvh) * 4 + s) * 8192 + tid * 16;
    v4u uu[1];
    poll_units<1, 0, 0>(rs, off, tag, uu);
    const v4u u = uu[0];
    *(LAS v2u*)(L + L_AI + (((tid >> 5) & 3) * 4 + (tid >> 7)) * 272 + j * 8) = v2u{u[0], u[2]};
  }
  STAMP(10);
  RAWBAR();
  STAMP(11);
  if (w < 4) {
    v4u af[4];
#pragma unroll
    for (int u = 0; u < 4; ++u) af[u] = *(const LAS v4u*)(L + L_AI + c * 272 + u * 64 + g * 16);
    f32x4 acc[8];
#pragma unroll
    for (int j = 0; j < 8; ++j) acc[j] = f32x4{0.f, 0.f, 0.f, 0.f};
#pragma unroll
    for (int u = 0; u < 4; ++u)
#pragma unroll
      for (int j = 0; j < 8; ++j) acc[j] = mfma(af[u], wf[8 * u + j], acc[j]);
    float y[8];
#pragma unroll
    for (int j = 0; j < 8; ++j) y[j] = (acc[j][0] + acc[j][1]) + (acc[j][2] + acc[j][3]);
    const int off = WS_Y + (b * 32 + 8 * w + (c >> 1)) * 8192 + ((h * 4 + g) * 4 + (c & 1) * 2) * 16;
    const v4u u0 = {cvtpk(y[0], y[1]), tag, cvtpk(y[2], y[3]), tag};
    const v4u u1 = {cvtpk(y[4], y[5]), tag, cvtpk(y[6], y[7]), tag};
    __builtin_amdgcn_raw_buffer_store_b128(u0, rs, off, 0, 16);
    __builtin_amdgcn_raw_buffer_store_b128(u1, rs, off + 16, 0, 16);
    STAMP(12);
  } else if (w == 4) {
    const int off = WS_Y + (b * 32 + h) * 8192 + g * 2048 + c * 16;
    v4u u[8];
    poll_units<8, 256, 0>(rs, off, tag, u);
    STAMP(12);
    float y0 = 0.f, y1 = 0.f, y2 = 0.f, y3 = 0.f;
#pragma unroll
    for (int i = 0; i < 8; ++i) { y0 += bflo(u[i][0]); y1 += bfhi(u[i][0]); y2 += bflo(u[i][2]); y3 += bfhi(u[i][2]); }
    y0 = xor32(xor16(y0, false), false); y1 = xor32(xor16(y1, false), false);
    y2 = xor32(xor16(y2, false), false); y3 = xor32(xor16(y3, false), false);
    if (g == 0) *(GAS v2u*)(out + (size_t)(b * 4 + (c >> 2)) * 512 + 16 * h + 4 * (c & 3)) = v2u{cvtpk(y0, y1), cvtpk(y2, y3)};
    if (h == 0 && lane == 0) __builtin_amdgcn_raw_buffer_store_b32(tag, rs, WS_EPOCH + b * 256, 0, 16);
    STAMP(13);
  }
  if (lane == 0) {
    unsigned long long* sp = (unsigned long long*)(ws + WS_STAMP) + (size_t)(bid * 8 + w) * 16;
#pragma unroll
    for (int i = 0; i < 16; ++i) sp[i] = st[i];
  }
}

__device__ __forceinline__ unsigned long long rt_() { unsigned long long t; asm volatile("s_memrealtime %0\n\ts_waitcnt lgkmcnt(0)" : "=s"(t) :: "memory"); return t; }
__global__ void k_pre(unsigned long long* st) { if (threadIdx.x == 0) st[2048 * 16] = rt_(); }
__global__ void __launch_bounds__(1024) k_delay(const unsigned long long* __restrict__ st, int which, int mult) {
  __shared__ unsigned long long smn[16], smx[16];
  const unsigned long long tstart = rt_();
  unsigned long long mn = ~0ull, mx = 0;
  for (int i = threadIdx.x; i < 2048; i += 1024) { mn = min(mn, st[i * 16]); mx = max(mx, st[i * 16 + 13]); }
  for (int o = 32; o; o >>= 1) { mn = min(mn, (unsigned long long)__shfl_xor((long long)mn, o)); mx = max(mx, (unsigned long long)__shfl_xor((long long)mx, o)); }
  if ((threadIdx.x & 63) == 0) { smn[threadIdx.x >> 6] = mn; smx[threadIdx.x >> 6] = mx; }
  __syncthreads();
  if (threadIdx.x >= 64) return;
  for (int i = 0; i < 16; ++i) { mn = min(mn, smn[i]); mx = max(mx, smx[i]); }
  unsigned long long span = which == 0 ? mx - mn : (which == 1 ? mn - st[2048 * 16] : tstart - mx);
  if (span > 10000) span = 10000;
  const unsigned long long t0 = rt_();
  while (rt_() - t0 < span * (unsigned long long)mult) {}
}
extern "C" void kernel_launch(void* const* d_in, const int* in_sizes, int n_in, void* d_out, int out_size, void* d_ws, size_t ws_size,
                              hipStream_t stream) {
  (void)hipFuncSetAttribute((const void*)gqa_fused, hipFuncAttributeMaxDynamicSharedMemorySize, L_END);
  gqa_fused<<<256, 512, L_END, stream>>>((const uint16_t*)d_in[0], (const uint16_t*)d_in[1], (const uint16_t*)d_in[2], (const uint16_t*)d_in[3],
                                         (const uint16_t*)d_in[4], (uint16_t*)d_out, (unsigned char*)d_ws);
  k_delay<<<1, 1024, 0, stream>>>((const unsigned long long*)((char*)d_ws + WS_STAMP), 0, 0);
}
```

```cpp
#include <hip/hip_runtime.h>
#include <stdint.h>

#define LAS __attribute__((address_space(3)))
#define GAS __attribute__((address_space(1)))
typedef unsigned v4u __attribute__((ext_vector_type(4)));
typedef unsigned v2u __attribute__((ext_vector_type(2)));
typedef float f32x4 __attribute__((ext_vector_type(4)));
typedef short bf16x8 __attribute__((ext_vector_type(8)));

#define KSIG 8
#define H2POS 1
#define DRY 0
#define CODEPF 1

constexpr int WS_EPOCH = 0;
constexpr int WS_Q = 4096;
constexpr int WS_STAT = WS_Q + 64 * 16384;
constexpr int WS_F1 = WS_STAT + 64 * 1024;
constexpr int WS_F2 = WS_F1 + 64 * 2048;
constexpr int WS_D1 = 2 << 20;
constexpr int WS_O = WS_D1 + 4096;
constexpr int WS_D2 = WS_O + 64 * 32768;
constexpr int WS_Y = WS_D2 + 4096;
constexpr int WS_D3 = WS_Y + 256 * 8192;
constexpr int WS_END = WS_D3 + 4096;
constexpr int WS_STAMP = WS_END;
static_assert(WS_F2 + 32 * 4096 <= WS_D1, "ws map");
constexpr int OOB = (int)0x80000000;
constexpr int L_K = 0;
constexpr int L_W = 32768;
constexpr int L_QP = 114688;
constexpr int L_P = 131072;
constexpr int L_QI = L_P + 16384;
constexpr int L_AI = L_QI + 4352;
constexpr int L_MAX = L_AI + 4352;
constexpr int L_SUM = L_MAX + 256;
constexpr int L_CNT = L_SUM + 256;
constexpr int L_END = L_CNT + 128;
enum { C_QP = 0, C_QI = 1, C_MAX = 2, C_P = 3, C_PW = 4  , C_KW = 8  , C_KI = 12, C_OA = 13, C_AI = 14, C_QR = 15, C_DUMMY = 16 };
constexpr unsigned SPIN_CAP = 1u << 18;

#define SB() __builtin_amdgcn_sched_barrier(0)
#define CB() asm volatile("" ::: "memory")

__device__ __forceinline__ f32x4 mfma(v4u a, v4u b, f32x4 c) {
  return __builtin_amdgcn_mfma_f32_16x16x32_bf16(__builtin_bit_cast(bf16x8, a), __builtin_bit_cast(bf16x8, b), c, 0, 0, 0);
}
__device__ __forceinline__ unsigned cvtpk(float lo, float hi) {
  typedef float f32x2_ __attribute__((ext_vector_type(2)));
  typedef __bf16 bf16x2_ __attribute__((ext_vector_type(2)));
  return __builtin_bit_cast(unsigned, __builtin_convertvector(f32x2_{lo, hi}, bf16x2_));
}
__device__ __forceinline__ void transpose8(const v4u* in, v4u* out) {
#pragma unroll
  for (int w = 0; w < 4; ++w) {
#pragma unroll
    for (int u = 0; u < 4; ++u) {
      out[2 * w][u] = __builtin_amdgcn_perm(in[2 * u + 1][w], in[2 * u][w], 0x05040100u);
      out[2 * w + 1][u] = __builtin_amdgcn_perm(in[2 * u + 1][w], in[2 * u][w], 0x07060302u);
    }
  }
}
__device__ __forceinline__ v4u ldnt(const void* p) { return __builtin_nontemporal_load((const GAS v4u*)p); }
__device__ __forceinline__ float xor16(float x, bool mx) {
  auto s = __builtin_amdgcn_permlane16_swap(__float_as_uint(x), __float_as_uint(x), false, false);
  return mx ? fmaxf(__uint_as_float(s[0]), __uint_as_float(s[1])) : __uint_as_float(s[0]) + __uint_as_float(s[1]);
}
__device__ __forceinline__ float xor32(float x, bool mx) {
  auto s = __builtin_amdgcn_permlane32_swap(__float_as_uint(x), __float_as_uint(x), false, false);
  return mx ? fmaxf(__uint_as_float(s[0]), __uint_as_float(s[1])) : __uint_as_float(s[0]) + __uint_as_float(s[1]);
}
template <int CTRL> __device__ __forceinline__ float dppf(float x) {
  return __builtin_bit_cast(float, __builtin_amdgcn_mov_dpp(__builtin_bit_cast(int, x), CTRL, 0xf, 0xf, true));
}
__device__ __forceinline__ float bflo(unsigned u) { return __uint_as_float(u << 16); }
__device__ __forceinline__ float bfhi(unsigned u) { return __uint_as_float(u & 0xffff0000u); }

__device__ __forceinline__ void lds_signal(LAS unsigned* cnt) {
  const unsigned addr = (unsigned)(uintptr_t)cnt, one = 1u;
  asm volatile("s_mov_b64 exec, 1\n\tds_add_u32 %0, %1\n\ts_mov_b64 exec, -1" :: "v"(addr), "v"(one) : "memory");
}
template <int SLEEP>
__device__ __forceinline__ void lds_wait(LAS unsigned* cnt, unsigned target) {
  CB();
  _Pragma("nounroll") for (unsigned spins = 0; spins < (SPIN_CAP << 4); ++spins) {
    const unsigned v = __builtin_amdgcn_readfirstlane(__hip_atomic_load(cnt, __ATOMIC_RELAXED, __HIP_MEMORY_SCOPE_WORKGROUP));
    if (v >= target) break;
    if (SLEEP) __builtin_amdgcn_s_sleep(SLEEP);
  }
  CB();
}
#define RAWBAR() do { SB(); asm volatile("s_waitcnt lgkmcnt(0)" ::: "memory"); __builtin_amdgcn_s_barrier(); CB(); SB(); } while (0)

__device__ __forceinline__ void dma16(const void* gsrc, unsigned lds_dst) {
  unsigned keep;
  asm volatile("s_mov_b32 %0, m0\n\ts_mov_b32 m0, %2\n\ts_nop 0\n\tglobal_load_lds_dwordx4 %1, off nt\n\ts_mov_b32 m0, %0" : "=&s"(keep) : "v"(gsrc), "s"(lds_dst) : "memory");
}
template <int N> __device__ __forceinline__ void waitvm() { asm volatile("s_waitcnt vmcnt(%0)" :: "i"(N) : "memory"); }
template <int N, int SLEEP>
__device__ __forceinline__ void poll_units(__amdgpu_buffer_rsrc_t rs, int (&o)[N], unsigned tag, v2u (&u)[N]) {
  _Pragma("nounroll") for (unsigned spins = 0; spins < SPIN_CAP; ++spins) {
    CB();
    v4u t[N];
#pragma unroll
    for (int i = 0; i < N; ++i) t[i] = __builtin_amdgcn_raw_buffer_load_b128(rs, o[i], 0, 16);
    bool pend = false;
#pragma unroll
    for (int i = 0; i < N; ++i) {
      const bool was = o[i] >= 0;
      const bool bad = was & ((t[i][1] != tag) | (t[i][3] != tag));
      u[i][0] = was ? t[i][0] : u[i][0];
      u[i][1] = was ? t[i][2] : u[i][1];
      o[i] = bad ? o[i] : OOB;
      pend |= bad;
    }
    if (!__any(pend)) break;
    if (SLEEP) __builtin_amdgcn_s_sleep(SLEEP);
  }
}
__device__ __forceinline__ void wait_hints(__amdgpu_buffer_rsrc_t rs, int fo, unsigned tag) {
  _Pragma("nounroll") for (unsigned spins = 0; spins < SPIN_CAP; ++spins) {
    CB();
    const unsigned f = __builtin_amdgcn_raw_buffer_load_b32(rs, fo, 0, 16);
    fo = (f == tag) ? OOB : fo;
    if (!__any(fo >= 0)) break;
  }
}

#define STAMP(i) do {} while (0)

__global__ void __launch_bounds__(512, 2) gqa_fused(const uint16_t* __restrict__ x, const uint16_t* __restrict__ kc, const uint16_t* __restrict__ vc,
                                                     const uint16_t* __restrict__ wq, const uint16_t* __restrict__ wo, uint16_t* out, unsigned char* ws) {
  extern __shared__ __attribute__((aligned(16))) unsigned char smem[];
  LAS unsigned char* L = (LAS unsigned char*)smem;
  LAS unsigned* cnt = (LAS unsigned*)(L + L_CNT);
  STAMP(0);
  const int tid = threadIdx.x, lane = tid & 63, w = __builtin_amdgcn_readfirstlane(tid >> 6), g = lane >> 4, c = lane & 15;
  const int bid = blockIdx.x, kvh = bid & 7, s = (bid >> 3) & 3, b = bid >> 5, G = b >> 2, cb = b & 3;
  unsigned cpf = 0;
  if (w == 7 && lane < 4) cpf = *(const GAS unsigned*)((__builtin_amdgcn_s_getpc() & ~127ull) + (size_t)((bid >> 3) * 4 + lane) * 128);
  const __amdgpu_buffer_rsrc_t rs = __builtin_amdgcn_make_buffer_rsrc((void*)ws, 0, WS_END, 0x00020000);
  const size_t kvbase = ((size_t)(b * 8 + kvh) * 2048 + s * 512) * 128;
  const int h = kvh * 4 + s;
  constexpr float C2 = 0.088388347648318440f * 1.4426950408889634f;
  const unsigned ep = __builtin_amdgcn_raw_buffer_load_b32(rs, WS_EPOCH + G * 256, 0, 16);

  if (w < 4) {
    const int hh = (cb * 4 + s) & 7, kh = cb >> 1;
    v4u xf[2], wr[8], wf[8];
    {
      const int k0 = 256 * kh + 64 * w + 32 * (c >> 3) + 8 * g;
      const uint16_t* p = wq + (size_t)k0 * 4096 + kvh * 512 + hh * 64 + 8 * (c & 7);
#pragma unroll
      for (int i = 0; i < 8; ++i) wr[i] = ldnt(p + (size_t)i * 4096);
#pragma unroll
      for (int e = 0; e < 2; ++e) xf[e] = *(const GAS v4u*)(x + (size_t)(16 * G + 8 * e + (c & 7)) * 512 + k0);
    }
    if (tid < 32) cnt[tid] = 0u;
    RAWBAR();
    {
      v4u fr[8];
      transpose8(wr, fr);
      LAS unsigned char* p = L + L_QP + ((w * 2 + (g >> 1)) * 16 + 4 * (g & 1)) * 256 + (c & 7) * 32;
#pragma unroll
      for (int e = 0; e < 2; ++e) {
        f32x4 acc[8];
#pragma unroll
        for (int j = 0; j < 8; ++j) acc[j] = mfma(xf[e], fr[j], f32x4{0.f, 0.f, 0.f, 0.f});
        if ((g >> 1) == (c >> 3)) {
#pragma unroll
          for (int r = 0; r < 4; ++r) {
            *(LAS f32x4*)(p + (8 * e + r) * 256) = f32x4{acc[0][r], acc[1][r], acc[2][r], acc[3][r]};
            *(LAS f32x4*)(p + (8 * e + r) * 256 + 16) = f32x4{acc[4][r], acc[5][r], acc[6][r], acc[7][r]};
          }
        }
      }
    }
    lds_signal(cnt + C_QP);
    STAMP(1);
    const unsigned tag = __builtin_amdgcn_readfirstlane(ep) + 1u;
    lds_wait<0>(cnt + C_QP, 4u);
    {
      const int r = tid >> 4, nq = tid & 15;
      f32x4 sum = {0.f, 0.f, 0.f, 0.f};
#pragma unroll
      for (int p = 0; p < 8; ++p) sum += *(const LAS f32x4*)(L + L_QP + (p * 16 + r) * 256 + nq * 16);
      lds_signal(cnt + C_QR);
      const v4u unit = {cvtpk(sum[0], sum[1]), tag, cvtpk(sum[2], sum[3]), tag};
      const int row = (hh >> 1) * 4 + (r & 3), dq = (hh & 1) * 16 + nq;
      __builtin_amdgcn_raw_buffer_store_b128(unit, rs, WS_Q + (((4 * G + w) * 8 + kvh) * 2 + kh) * 8192 + (row * 32 + dq) * 16, 0, 16);
    }
    STAMP(2);
    {
      const uint16_t* wop = wo + (size_t)(h * 128 + 32 * w + 8 * g) * 512 + 128 * cb + 8 * c;
#pragma unroll
      for (int i = 0; i < 8; ++i) wf[i] = ldnt(wop + (size_t)i * 512);
    }
    SB();
    lds_wait<2>(cnt + C_KI, 4u);
    STAMP(3);
    {
      int o[4]; v2u u[4];
#pragma unroll
      for (int i = 0; i < 4; ++i) o[i] = WS_Q + (b * 8 + kvh) * 16384 + i * 4096 + tid * 16;
      poll_units<4, 2>(rs, o, tag, u);
#pragma unroll
      for (int i = 0; i < 2; ++i)
        *(LAS v2u*)(L + L_QI + ((tid >> 5) + 8 * i) * 272 + (tid & 31) * 8) =
            v2u{cvtpk(bflo(u[i][0]) + bflo(u[i + 2][0]), bfhi(u[i][0]) + bfhi(u[i + 2][0])), cvtpk(bflo(u[i][1]) + bflo(u[i + 2][1]), bfhi(u[i][1]) + bfhi(u[i + 2][1]))};
    }
    lds_signal(cnt + C_QI);
    STAMP(4);
    v4u wt[8];
    transpose8(wf, wt);
    lds_wait<1>(cnt + C_KW + w, 1u);
    STAMP(14);
    v4u kf[32];
    {
      const int krow = 4 * (c & 3) + (c >> 2);
#pragma unroll
      for (int t = 0; t < 8; ++t)
#pragma unroll
        for (int stp = 0; stp < 4; ++stp) kf[t * 4 + stp] = *(const LAS v4u*)(L + L_K + (w + 4 * t) * 4096 + krow * 256 + (((4 * stp + g) ^ c) << 4));
    }
    SB();
    lds_wait<0>(cnt + C_QI, 4u);
    v4u qf[4];
#pragma unroll
    for (int stp = 0; stp < 4; ++stp) qf[stp] = *(const LAS v4u*)(L + L_QI + c * 272 + stp * 64 + g * 16);
    f32x4 sc[8];
#pragma unroll
    for (int t = 0; t < 8; ++t) sc[t] = f32x4{0.f, 0.f, 0.f, 0.f};
#pragma unroll
    for (int stp = 0; stp < 4; ++stp)
#pragma unroll
      for (int t = 0; t < 8; ++t) sc[t] = mfma(kf[t * 4 + stp], qf[stp], sc[t]);
    float m = sc[0][0];
#pragma unroll
    for (int t = 0; t < 8; ++t)
#pragma unroll
      for (int r = 0; r < 4; ++r) m = fmaxf(m, sc[t][r]);
    m = xor32(xor16(m, true), true);
    if (g == 0) *(LAS float*)(L + L_MAX + (w * 16 + c) * 4) = m;
    lds_signal(cnt + C_MAX);
    STAMP(15);
    lds_wait<0>(cnt + C_MAX, 4u);
#pragma unroll
    for (int ww = 0; ww < 4; ++ww) m = fmaxf(m, *(const LAS float*)(L + L_MAX + (ww * 16 + c) * 4));
    const float ms = m * C2;
    float l0 = 0.f, l1 = 0.f;
#pragma unroll
    for (int t = 0; t < 8; ++t) {
#pragma unroll
      for (int r = 0; r < 4; ++r) sc[t][r] = __builtin_amdgcn_exp2f(fmaf(sc[t][r], C2, -ms));
      l0 += sc[t][0] + sc[t][1]; l1 += sc[t][2] + sc[t][3];
    }
#pragma unroll
    for (int jj = 0; jj < 4; ++jj) {
      const v4u pf = {cvtpk(sc[2 * jj][0], sc[2 * jj][1]), cvtpk(sc[2 * jj][2], sc[2 * jj][3]), cvtpk(sc[2 * jj + 1][0], sc[2 * jj + 1][1]), cvtpk(sc[2 * jj + 1][2], sc[2 * jj + 1][3])};
      *(LAS v4u*)(L + L_P + (w * 4 + jj) * 1024 + lane * 16) = pf;
    }
    lds_signal(cnt + C_PW + w);
    const float l = xor32(xor16(l0 + l1, false), false);
    if (g == 0) *(LAS float*)(L + L_SUM + (w * 16 + c) * 4) = l;
#pragma unroll
    for (int j = 0; j < 8; ++j) *(LAS v4u*)(L + L_W + (j * 4 + w) * 1056 + lane * 16) = wt[j];
    lds_signal(cnt + C_P);
    STAMP(5);
    lds_wait<0>(cnt + C_P, 4u);
    v4u bw[2][4];
#pragma unroll
    for (int mm = 0; mm < 2; ++mm)
#pragma unroll
      for (int u = 0; u < 4; ++u) bw[mm][u] = *(const LAS v4u*)(L + L_W + ((c & 7) * 4 + u) * 1056 + (g * 16 + 2 * (2 * w + mm) + (c >> 3)) * 16);
    if (w == 0) {
      float L4 = 0.f;
#pragma unroll
      for (int ww = 0; ww < 4; ++ww) L4 += *(const LAS float*)(L + L_SUM + (ww * 16 + c) * 4);
      if (g == 0) {
        const v4u unit = {__float_as_uint(ms), tag, __float_as_uint(L4), tag};
        __builtin_amdgcn_raw_buffer_store_b128(unit, rs, WS_STAT + (G * 32 + kvh * 4 + (c >> 2)) * 1024 + ((4 * cb + (c & 3)) * 4 + s) * 16, 0, 16);
      }
    }
    STAMP(6);
    int pass = DRY ? 0 : 1;
    asm volatile("" : "+s"(pass));
    _Pragma("nounroll") for (; pass < 2; ++pass) {
      const int msk = pass ? 0 : OOB;
      if (pass == 0) {
        const unsigned t0 = __builtin_amdgcn_raw_buffer_load_b32(rs, lane == 0 ? WS_D1 : (lane == 1 ? WS_D2 : (lane == 2 ? WS_D3 : OOB)), 0, 0);
        asm volatile("" :: "v"(t0));
      }
      lds_wait<0>(cnt + C_OA, pass ? 4u : 0u);
      STAMP(7);
#pragma unroll
      for (int hf = 0; hf < 2; ++hf) {
        const int pos = (lane >> 5) + 2 * hf, row = 4 * w + pos, j = lane & 31;
        f32x4 o = {0.f, 0.f, 0.f, 0.f};
#pragma unroll
        for (int aa = 0; aa < 4; ++aa) o += *(const LAS f32x4*)(L + L_K + (aa + 4 * (row >> 3)) * 4096 + (row & 7) * 512 + ((j ^ (row & 7)) << 4));
        const v4u unit = {cvtpk(o[0], o[1]), tag, cvtpk(o[2], o[3]), tag};
        __builtin_amdgcn_raw_buffer_store_b128(unit, rs, (WS_O + (G * 32 + kvh * 4 + w) * 32768 + (4 * cb + pos) * 2048 + s * 512 + j * 16) | msk, 0, 16);
      }
      __builtin_amdgcn_raw_buffer_store_b32(tag, rs, lane == 0 ? (WS_F1 + (G * 32 + kvh * 4 + w) * 2048 + (cb * 4 + s) * 128) | msk : OOB, 0, 16);
      STAMP(8);
      wait_hints(rs, lane < 4 ? (WS_F1 + (G * 32 + h) * 2048 + (w * 4 + lane) * 128) | msk : OOB, tag);
      STAMP(9);
      int o[9]; v2u u[9];
      o[0] = lane < 16 ? (WS_STAT + (G * 32 + h) * 1024 + (w * 16 + lane) * 16) | msk : OOB;
#pragma unroll
      for (int i = 0; i < 9; ++i) u[i] = v2u{0u, 0x3f800000u};
#pragma unroll
      for (int i = 0; i < 8; ++i) o[1 + i] = (WS_O + (G * 32 + h) * 32768 + (4 * w + (lane >> 5) + 2 * (i >> 2)) * 2048 + (i & 3) * 512 + (lane & 31) * 16) | msk;
      poll_units<9, 0>(rs, o, tag, u);
      STAMP(10);
      const float mo = __uint_as_float(u[0][0]), lo = __uint_as_float(u[0][1]);
      float M = fmaxf(mo, dppf<0xB1>(mo)); M = fmaxf(M, dppf<0x4E>(M));
      const float e = __builtin_amdgcn_exp2f(mo - M);
      float Lt = e * lo; Lt += dppf<0xB1>(Lt); Lt += dppf<0x4E>(Lt);
      const int wv = __float_as_int(e * __builtin_amdgcn_rcpf(Lt));
#pragma unroll
      for (int hf = 0; hf < 2; ++hf) {
        float a0 = 0.f, a1 = 0.f, a2 = 0.f, a3 = 0.f;
#pragma unroll
        for (int sr = 0; sr < 4; ++sr) {
          const float w0 = __int_as_float(__builtin_amdgcn_readlane(wv, 8 * hf + sr)), w1 = __int_as_float(__builtin_amdgcn_readlane(wv, 8 * hf + 4 + sr));
          const float wg = lane < 32 ? w0 : w1;
          const v2u uu = u[1 + 4 * hf + sr];
          a0 = fmaf(wg, bflo(uu[0]), a0); a1 = fmaf(wg, bfhi(uu[0]), a1);
          a2 = fmaf(wg, bflo(uu[1]), a2); a3 = fmaf(wg, bfhi(uu[1]), a3);
        }
        *(LAS v2u*)(L + L_AI + (4 * w + (lane >> 5) + 2 * hf) * 272 + (lane & 31) * 8) = v2u{cvtpk(a0, a1), cvtpk(a2, a3)};
      }
      lds_signal(cnt + (pass ? C_AI : C_DUMMY));
      lds_wait<0>(cnt + C_AI, pass ? 4u : 0u);
      const int f2 = lane == 0 ? (WS_F2 + (((G * 4 + cb) * 4 + w) * 32 + h) * 128) | msk : OOB;
      if (H2POS == 1) __builtin_amdgcn_raw_buffer_store_b32(tag, rs, f2, 0, 16);
      STAMP(11);
      v4u af[4];
#pragma unroll
      for (int u2 = 0; u2 < 4; ++u2) af[u2] = *(const LAS v4u*)(L + L_AI + c * 272 + u2 * 64 + g * 16);
#pragma unroll
      for (int mm = 0; mm < 2; ++mm) {
        f32x4 y = {0.f, 0.f, 0.f, 0.f};
#pragma unroll
        for (int u2 = 0; u2 < 4; ++u2) y = mfma(af[u2], bw[mm][u2], y);
        const v4u unit = {cvtpk(y[0], y[1]), tag, cvtpk(y[2], y[3]), tag};
        __builtin_amdgcn_raw_buffer_store_b128(unit, rs, (WS_Y + ((G * 4 + g) * 32 + 8 * cb + 2 * w + mm) * 8192 + (h * 16 + c) * 16) | msk, 0, 16);
      }
      if (H2POS == 2) __builtin_amdgcn_raw_buffer_store_b32(tag, rs, f2, 0, 16);
      STAMP(12);
    }
  } else {
    const int a = w - 4;
    const uint16_t* vp = vc + kvbase + a * 2048 + g * 128 + 8 * c;
    v4u vf[32];
    {
      const unsigned char* kp = (const unsigned char*)(kc + kvbase) + a * 4096 + g * 256;
      const unsigned lb = (unsigned)(uintptr_t)(L + L_K) + a * 4096;
#pragma unroll
      for (int t = 0; t < 8; ++t) {
        if (t == 7) lds_wait<1>(cnt + C_QR, 4u);
#pragma unroll
        for (int i = 0; i < 4; ++i) dma16(kp + t * 16384 + i * 1024 + ((c ^ (4 * g + i)) << 4), lb + t * 16384 + i * 1024);
        if (t == 0) RAWBAR();
      }
    }
    lds_signal(cnt + C_KI);
    STAMP(2);
#pragma unroll
    for (int i = 0; i < KSIG; ++i) vf[i] = ldnt(vp + (i >> 2) * 8192 + (i & 3) * 512);
    SB();
    waitvm<KSIG>();
    lds_signal(cnt + C_KW + a);
    SB();
    STAMP(4);
#pragma unroll
    for (int i = KSIG; i < 32; ++i) vf[i] = ldnt(vp + (i >> 2) * 8192 + (i & 3) * 512);
    SB();
    STAMP(3);
    const unsigned tag = __builtin_amdgcn_readfirstlane(ep) + 1u;
    lds_wait<2>(cnt + C_PW + a, 1u);
    STAMP(5);
    v4u pf[4];
#pragma unroll
    for (int jj = 0; jj < 4; ++jj) pf[jj] = *(const LAS v4u*)(L + L_P + (a * 4 + jj) * 1024 + lane * 16);
    f32x4 acc[8];
#pragma unroll
    for (int j = 0; j < 8; ++j) acc[j] = f32x4{0.f, 0.f, 0.f, 0.f};
#pragma unroll
    for (int jj = 0; jj < 4; ++jj) {
      v4u fr[8];
      transpose8(vf + 8 * jj, fr);
#pragma unroll
      for (int j = 0; j < 8; ++j) acc[j] = mfma(fr[j], pf[jj], acc[j]);
    }
    STAMP(6);
    {
      LAS unsigned char* p = L + L_K + (a + 4 * (c >> 3)) * 4096 + (c & 7) * 512;
#pragma unroll
      for (int r = 0; r < 4; ++r) {
        *(LAS f32x4*)(p + (((8 * g + 2 * r) ^ (c & 7)) << 4)) = f32x4{acc[0][r], acc[1][r], acc[2][r], acc[3][r]};
        *(LAS f32x4*)(p + (((8 * g + 2 * r + 1) ^ (c & 7)) << 4)) = f32x4{acc[4][r], acc[5][r], acc[6][r], acc[7][r]};
      }
    }
    lds_signal(cnt + C_OA);
    STAMP(7);
    if (a == 0) {
      const __amdgpu_buffer_rsrc_t ro = __builtin_amdgcn_make_buffer_rsrc((void*)out, 0, 32768, 0x00020000);
      int pass = DRY ? 0 : 1;
      asm volatile("" : "+s"(pass));
      _Pragma("nounroll") for (; pass < 2; ++pass) {
        const int msk = pass ? 0 : OOB;
        wait_hints(rs, lane < 32 ? (WS_F2 + (((G * 4 + (h >> 3)) * 4 + ((h & 7) >> 1)) * 32 + lane) * 128) | msk : OOB, tag);
        STAMP(11);
        int o[8]; v2u u[8];
#pragma unroll
        for (int i = 0; i < 8; ++i) { o[i] = (WS_Y + (b * 32 + h) * 8192 + g * 2048 + i * 256 + c * 16) | msk; u[i] = v2u{0u, 0u}; }
        poll_units<8, 0>(rs, o, tag, u);
        STAMP(12);
        float y0 = 0.f, y1 = 0.f, y2 = 0.f, y3 = 0.f;
#pragma unroll
        for (int i = 0; i < 8; ++i) { y0 += bflo(u[i][0]); y1 += bfhi(u[i][0]); y2 += bflo(u[i][1]); y3 += bfhi(u[i][1]); }
        y0 = xor32(xor16(y0, false), false); y1 = xor32(xor16(y1, false), false);
        y2 = xor32(xor16(y2, false), false); y3 = xor32(xor16(y3, false), false);
        const float y = g == 0 ? y0 : (g == 1 ? y1 : (g == 2 ? y2 : y3));
        __builtin_amdgcn_raw_buffer_store_b16((unsigned short)cvtpk(y, y), ro, (((b * 4 + g) * 512 + 16 * h + c) * 2) | msk, 0, 0);
        __builtin_amdgcn_raw_buffer_store_b32(tag, rs, (cb == 0 && h == 0 && lane == 0) ? (WS_EPOCH + G * 256) | msk : OOB, 0, 16);
        STAMP(13);
      }
    }
  }
  asm volatile("" :: "v"(cpf));
}

extern "C" void kernel_launch(void* const* d_in, const int* in_sizes, int n_in, void* d_out, int out_size, void* d_ws, size_t ws_size,
                              hipStream_t stream) {
  (void)hipFuncSetAttribute((const void*)gqa_fused, hipFuncAttributeMaxDynamicSharedMemorySize, L_END);
  gqa_fused<<<256, 512, L_END, stream>>>((const uint16_t*)d_in[0], (const uint16_t*)d_in[1], (const uint16_t*)d_in[2], (const uint16_t*)d_in[3],
                                         (const uint16_t*)d_in[4], (uint16_t*)d_out, (unsigned char*)d_ws);
}
```

```cpp
#include <hip/hip_runtime.h>
#include <stdint.h>

#define LAS __attribute__((address_space(3)))
#define GAS __attribute__((address_space(1)))
typedef unsigned v4u __attribute__((ext_vector_type(4)));
typedef unsigned v2u __attribute__((ext_vector_type(2)));
typedef float f32x4 __attribute__((ext_vector_type(4)));
typedef short bf16x8 __attribute__((ext_vector_type(8)));

#define KSIG 8
#define WOLATE 1
#define APOLL 0
#define H2MODE 0
#define CPFN 25
#define PRET 1
#define CODEPF 1

constexpr int WS_EPOCH = 0;
constexpr int WS_Q = 4096;
constexpr int WS_STAT = WS_Q + 64 * 16384;
constexpr int WS_F1 = WS_STAT + 64 * 1024;
constexpr int WS_F2 = WS_F1 + 64 * 2048;
constexpr int WS_D1 = 2 << 20;
constexpr int WS_O = WS_D1 + 4096;
constexpr int WS_D2 = WS_O + 64 * 32768;
constexpr int WS_Y = WS_D2 + 4096;
constexpr int WS_D3 = WS_Y + 256 * 8192;
constexpr int WS_END = WS_D3 + 4096;
constexpr int WS_STAMP = WS_END;
static_assert(WS_F2 + 32 * 4096 <= WS_D1, "ws map");
constexpr int OOB = (int)0x80000000;
constexpr int L_K = 0;
constexpr int L_W = 32768;
constexpr int L_QP = 114688;
constexpr int L_P = 131072;
constexpr int L_QI = L_P + 16384;
constexpr int L_AI = L_QI + 4352;
constexpr int L_MAX = L_AI + 4352;
constexpr int L_SUM = L_MAX + 256;
constexpr int L_CNT = L_SUM + 256;
constexpr int L_END = L_CNT + 128;
enum { C_QP = 0, C_QI = 1, C_MAX = 2, C_P = 3, C_PW = 4  , C_KW = 8  , C_KI = 12, C_OA = 13, C_AI = 14, C_QR = 15, C_VI = 16, C_W = 17, C_Y = 18 };
constexpr unsigned SPIN_CAP = 1u << 18;

#define SB() __builtin_amdgcn_sched_barrier(0)
#define CB() asm volatile("" ::: "memory")

__device__ __forceinline__ f32x4 mfma(v4u a, v4u b, f32x4 c) {
  return __builtin_amdgcn_mfma_f32_16x16x32_bf16(__builtin_bit_cast(bf16x8, a), __builtin_bit_cast(bf16x8, b), c, 0, 0, 0);
}
__device__ __forceinline__ unsigned cvtpk(float lo, float hi) {
  typedef float f32x2_ __attribute__((ext_vector_type(2)));
  typedef __bf16 bf16x2_ __attribute__((ext_vector_type(2)));
  return __builtin_bit_cast(unsigned, __builtin_convertvector(f32x2_{lo, hi}, bf16x2_));
}
__device__ __forceinline__ void transpose8(const v4u* in, v4u* out) {
#pragma unroll
  for (int w = 0; w < 4; ++w) {
#pragma unroll
    for (int u = 0; u < 4; ++u) {
      out[2 * w][u] = __builtin_amdgcn_perm(in[2 * u + 1][w], in[2 * u][w], 0x05040100u);
      out[2 * w + 1][u] = __builtin_amdgcn_perm(in[2 * u + 1][w], in[2 * u][w], 0x07060302u);
    }
  }
}
__device__ __forceinline__ v4u ldnt(const void* p) { return __builtin_nontemporal_load((const GAS v4u*)p); }
#define WPOL 1
__device__ __forceinline__ v4u ldw(const void* p) { return WPOL ? *(const GAS v4u*)p : __builtin_nontemporal_load((const GAS v4u*)p); }
__device__ __forceinline__ float xor16(float x, bool mx) {
  auto s = __builtin_amdgcn_permlane16_swap(__float_as_uint(x), __float_as_uint(x), false, false);
  return mx ? fmaxf(__uint_as_float(s[0]), __uint_as_float(s[1])) : __uint_as_float(s[0]) + __uint_as_float(s[1]);
}
__device__ __forceinline__ float xor32(float x, bool mx) {
  auto s = __builtin_amdgcn_permlane32_swap(__float_as_uint(x), __float_as_uint(x), false, false);
  return mx ? fmaxf(__uint_as_float(s[0]), __uint_as_float(s[1])) : __uint_as_float(s[0]) + __uint_as_float(s[1]);
}
template <int CTRL> __device__ __forceinline__ float dppf(float x) {
  return __builtin_bit_cast(float, __builtin_amdgcn_mov_dpp(__builtin_bit_cast(int, x), CTRL, 0xf, 0xf, true));
}
__device__ __forceinline__ float bflo(unsigned u) { return __uint_as_float(u << 16); }
__device__ __forceinline__ float bfhi(unsigned u) { return __uint_as_float(u & 0xffff0000u); }

__device__ __forceinline__ void lds_signal(LAS unsigned* cnt) {
  const unsigned addr = (unsigned)(uintptr_t)cnt, one = 1u;
  asm volatile("s_mov_b64 exec, 1\n\tds_add_u32 %0, %1\n\ts_mov_b64 exec, -1" :: "v"(addr), "v"(one) : "memory");
}
template <int SLEEP>
__device__ __forceinline__ void lds_wait(LAS unsigned* cnt, unsigned target) {
  CB();
  _Pragma("nounroll") for (unsigned spins = 0; spins < (SPIN_CAP << 4); ++spins) {
    const unsigned v = __builtin_amdgcn_readfirstlane(__hip_atomic_load(cnt, __ATOMIC_RELAXED, __HIP_MEMORY_SCOPE_WORKGROUP));
    if (v >= target) break;
    if (SLEEP) __builtin_amdgcn_s_sleep(SLEEP);
  }
  CB();
}
#define RAWBAR() do { SB(); asm volatile("s_waitcnt lgkmcnt(0)" ::: "memory"); __builtin_amdgcn_s_barrier(); CB(); SB(); } while (0)

__device__ __forceinline__ void dma16(const void* gsrc, unsigned lds_dst) {
  unsigned keep;
  asm volatile("s_mov_b32 %0, m0\n\ts_mov_b32 m0, %2\n\ts_nop 0\n\tglobal_load_lds_dwordx4 %1, off nt\n\ts_mov_b32 m0, %0" : "=&s"(keep) : "v"(gsrc), "s"(lds_dst) : "memory");
}
template <int N> __device__ __forceinline__ void waitvm() { asm volatile("s_waitcnt vmcnt(%0)" :: "i"(N) : "memory"); }
template <int N, int SLEEP>
__device__ __forceinline__ void poll_units(__amdgpu_buffer_rsrc_t rs, int (&o)[N], unsigned tag, v2u (&u)[N]) {
  _Pragma("nounroll") for (unsigned spins = 0; spins < SPIN_CAP; ++spins) {
    CB();
    v4u t[N];
#pragma unroll
    for (int i = 0; i < N; ++i) t[i] = __builtin_amdgcn_raw_buffer_load_b128(rs, o[i], 0, 16);
    bool pend = false;
#pragma unroll
    for (int i = 0; i < N; ++i) {
      const bool was = o[i] >= 0;
      const bool bad = was & ((t[i][1] != tag) | (t[i][3] != tag));
      u[i][0] = was ? t[i][0] : u[i][0];
      u[i][1] = was ? t[i][2] : u[i][1];
      o[i] = bad ? o[i] : OOB;
      pend |= bad;
    }
    if (!__any(pend)) break;
    if (SLEEP) __builtin_amdgcn_s_sleep(SLEEP);
  }
}
template <bool ATOMIC>
__device__ __forceinline__ void wait_hints(__amdgpu_buffer_rsrc_t rs, int fo, unsigned tag) {
  _Pragma("nounroll") for (unsigned spins = 0; spins < SPIN_CAP; ++spins) {
    CB();
    unsigned f;
    if (ATOMIC) f = __builtin_amdgcn_raw_ptr_buffer_atomic_add_i32(0, rs, fo, 0, 0);
    else f = __builtin_amdgcn_raw_buffer_load_b32(rs, fo, 0, 16);
    fo = (f == tag) ? OOB : fo;
    if (!__any(fo >= 0)) break;
  }
}

#define STAMP(i) do {} while (0)

__global__ void __launch_bounds__(512, 2) gqa_fused(const uint16_t* __restrict__ x, const uint16_t* __restrict__ kc, const uint16_t* __restrict__ vc,
                                                     const uint16_t* __restrict__ wq, const uint16_t* __restrict__ wo, uint16_t* out, unsigned char* ws) {
  extern __shared__ __attribute__((aligned(16))) unsigned char smem[];
  LAS unsigned char* L = (LAS unsigned char*)smem;
  LAS unsigned* cnt = (LAS unsigned*)(L + L_CNT);
  STAMP(0);
  const int tid = threadIdx.x, lane = tid & 63, w = __builtin_amdgcn_readfirstlane(tid >> 6), g = lane >> 4, c = lane & 15;
  const int bid = blockIdx.x, kvh = bid & 7, s = (bid >> 3) & 3, b = bid >> 5, G = b >> 2, cb = b & 3;
  unsigned cpf = 0;
  if (w == 7 && lane < 4 && (bid >> 3) < CPFN) cpf = *(const GAS unsigned*)((__builtin_amdgcn_s_getpc() & ~127ull) + (size_t)((bid >> 3) * 4 + lane) * 128);
  const __amdgpu_buffer_rsrc_t rs = __builtin_amdgcn_make_buffer_rsrc((void*)ws, 0, WS_END, 0x00020000);
  const size_t kvbase = ((size_t)(b * 8 + kvh) * 2048 + s * 512) * 128;
  const int h = kvh * 4 + s;
  constexpr float C2 = 0.088388347648318440f * 1.4426950408889634f;
  const unsigned ep = __builtin_amdgcn_raw_buffer_load_b32(rs, WS_EPOCH + G * 256, 0, 16);

  const int hh = (cb * 4 + s) & 7, kh = cb >> 1;
  v4u xf[2], wr[8];
  const unsigned char* kp = (const unsigned char*)(kc + kvbase) + (w & 3) * 4096 + g * 256;
  const unsigned lb = (unsigned)(uintptr_t)(L + L_K) + (w & 3) * 4096;
  if (w < 4) {
    const int k0 = 256 * kh + 64 * w + 32 * (c >> 3) + 8 * g;
    const uint16_t* p = wq + (size_t)k0 * 4096 + kvh * 512 + hh * 64 + 8 * (c & 7);
#pragma unroll
    for (int i = 0; i < 8; ++i) wr[i] = ldw(p + (size_t)i * 4096);
#pragma unroll
    for (int e = 0; e < 2; ++e) xf[e] = *(const GAS v4u*)(x + (size_t)(16 * G + 8 * e + (c & 7)) * 512 + k0);
    if (tid < 32) cnt[tid] = 0u;
  } else {
#pragma unroll
    for (int t = 0; t < PRET; ++t)
#pragma unroll
      for (int i = 0; i < 4; ++i) dma16(kp + t * 16384 + i * 1024 + ((c ^ (4 * g + i)) << 4), lb + t * 16384 + i * 1024);
  }
  RAWBAR();

  if (w < 4) {
    v4u wf[8];
    SB();
    {
      v4u fr[8];
      transpose8(wr, fr);
      LAS unsigned char* p = L + L_QP + ((w * 2 + (g >> 1)) * 16 + 4 * (g & 1)) * 256 + (c & 7) * 32;
#pragma unroll
      for (int e = 0; e < 2; ++e) {
        f32x4 acc[8];
#pragma unroll
        for (int j = 0; j < 8; ++j) acc[j] = mfma(xf[e], fr[j], f32x4{0.f, 0.f, 0.f, 0.f});
        if ((g >> 1) == (c >> 3)) {
#pragma unroll
          for (int r = 0; r < 4; ++r) {
            *(LAS f32x4*)(p + (8 * e + r) * 256) = f32x4{acc[0][r], acc[1][r], acc[2][r], acc[3][r]};
            *(LAS f32x4*)(p + (8 * e + r) * 256 + 16) = f32x4{acc[4][r], acc[5][r], acc[6][r], acc[7][r]};
          }
        }
      }
    }
    lds_signal(cnt + C_QP);
    STAMP(1);
    const unsigned tag = __builtin_amdgcn_readfirstlane(ep) + 1u;
    lds_wait<0>(cnt + C_QP, 4u);
    {
      const int r = tid >> 4, nq = tid & 15;
      f32x4 sum = {0.f, 0.f, 0.f, 0.f};
#pragma unroll
      for (int p = 0; p < 8; ++p) sum += *(const LAS f32x4*)(L + L_QP + (p * 16 + r) * 256 + nq * 16);
      lds_signal(cnt + C_QR);
      const v4u unit = {cvtpk(sum[0], sum[1]), tag, cvtpk(sum[2], sum[3]), tag};
      const int row = (hh >> 1) * 4 + (r & 3), dq = (hh & 1) * 16 + nq;
      __builtin_amdgcn_raw_buffer_store_b128(unit, rs, WS_Q + (((4 * G + w) * 8 + kvh) * 2 + kh) * 8192 + (row * 32 + dq) * 16, 0, 16);
    }
    STAMP(2);
    const uint16_t* wop = wo + (size_t)(h * 128 + 32 * w + 2 * g) * 512 + 128 * cb + 8 * c;
#define WO_LOAD() _Pragma("unroll") for (int i = 0; i < 8; ++i) wf[i] = ldw(wop + (size_t)(8 * (i & 3) + (i >> 2)) * 512)
    if (!WOLATE) { WO_LOAD(); }
    SB();
    lds_wait<2>(cnt + C_KI, 4u);
    {
      int o[4]; v2u u[4];
#pragma unroll
      for (int i = 0; i < 4; ++i) o[i] = WS_Q + (b * 8 + kvh) * 16384 + i * 4096 + tid * 16;
      poll_units<4, 2>(rs, o, tag, u);
#pragma unroll
      for (int i = 0; i < 2; ++i)
        *(LAS v2u*)(L + L_QI + ((tid >> 5) + 8 * i) * 272 + (tid & 31) * 8) =
            v2u{cvtpk(bflo(u[i][0]) + bflo(u[i + 2][0]), bfhi(u[i][0]) + bfhi(u[i + 2][0])), cvtpk(bflo(u[i][1]) + bflo(u[i + 2][1]), bfhi(u[i][1]) + bfhi(u[i + 2][1]))};
    }
    lds_signal(cnt + C_QI);
    STAMP(4);
    lds_wait<1>(cnt + C_KW + w, 1u);
    STAMP(14);
    v4u kf[32];
    {
      const int krow = 4 * (c & 3) + (c >> 2);
#pragma unroll
      for (int t = 0; t < 8; ++t)
#pragma unroll
        for (int stp = 0; stp < 4; ++stp) kf[t * 4 + stp] = *(const LAS v4u*)(L + L_K + (w + 4 * t) * 4096 + krow * 256 + (((4 * stp + g) ^ c) << 4));
    }
    SB();
    lds_wait<0>(cnt + C_QI, 4u);
    v4u qf[4];
#pragma unroll
    for (int stp = 0; stp < 4; ++stp) qf[stp] = *(const LAS v4u*)(L + L_QI + c * 272 + stp * 64 + g * 16);
    f32x4 sc[8];
#pragma unroll
    for (int t = 0; t < 8; ++t) sc[t] = f32x4{0.f, 0.f, 0.f, 0.f};
#pragma unroll
    for (int stp = 0; stp < 4; ++stp)
#pragma unroll
      for (int t = 0; t < 8; ++t) sc[t] = mfma(kf[t * 4 + stp], qf[stp], sc[t]);
    float m = sc[0][0];
#pragma unroll
    for (int t = 0; t < 8; ++t)
#pragma unroll
      for (int r = 0; r < 4; ++r) m = fmaxf(m, sc[t][r]);
    m = xor32(xor16(m, true), true);
    if (g == 0) *(LAS float*)(L + L_MAX + (w * 16 + c) * 4) = m;
    lds_signal(cnt + C_MAX);
    STAMP(15);
    lds_wait<0>(cnt + C_MAX, 4u);
#pragma unroll
    for (int ww = 0; ww < 4; ++ww) m = fmaxf(m, *(const LAS float*)(L + L_MAX + (ww * 16 + c) * 4));
    const float ms = m * C2;
    float l0 = 0.f, l1 = 0.f;
#pragma unroll
    for (int t = 0; t < 8; ++t) {
#pragma unroll
      for (int r = 0; r < 4; ++r) sc[t][r] = __builtin_amdgcn_exp2f(fmaf(sc[t][r], C2, -ms));
      l0 += sc[t][0] + sc[t][1]; l1 += sc[t][2] + sc[t][3];
    }
#pragma unroll
    for (int jj = 0; jj < 4; ++jj) {
      const v4u pf = {cvtpk(sc[2 * jj][0], sc[2 * jj][1]), cvtpk(sc[2 * jj][2], sc[2 * jj][3]), cvtpk(sc[2 * jj + 1][0], sc[2 * jj + 1][1]), cvtpk(sc[2 * jj + 1][2], sc[2 * jj + 1][3])};
      *(LAS v4u*)(L + L_P + (w * 4 + jj) * 1024 + lane * 16) = pf;
    }
    lds_signal(cnt + C_PW + w);
    const float l = xor32(xor16(l0 + l1, false), false);
    if (g == 0) *(LAS float*)(L + L_SUM + (w * 16 + c) * 4) = l;
    lds_signal(cnt + C_P);
    STAMP(5);
    v4u bw[2][4];
#define WO_REGROUP() do { v4u wt[8]; transpose8(wf, wt); \
      _Pragma("unroll") for (int j = 0; j < 8; ++j) *(LAS v4u*)(L + L_W + (j * 4 + w) * 1056 + lane * 16) = wt[j]; \
      lds_signal(cnt + C_W); lds_wait<0>(cnt + C_W, 4u); \
      _Pragma("unroll") for (int mm = 0; mm < 2; ++mm) _Pragma("unroll") for (int u = 0; u < 4; ++u) \
        bw[mm][u] = *(const LAS v4u*)(L + L_W + ((c & 7) * 4 + u) * 1056 + (g * 16 + 2 * (2 * w + mm) + (c >> 3)) * 16); } while (0)
    if (!WOLATE) WO_REGROUP();
    if (w == 0) {
      lds_wait<0>(cnt + C_P, 4u);
      float L4 = 0.f;
#pragma unroll
      for (int ww = 0; ww < 4; ++ww) L4 += *(const LAS float*)(L + L_SUM + (ww * 16 + c) * 4);
      if (g == 0) {
        const v4u unit = {__float_as_uint(ms), tag, __float_as_uint(L4), tag};
        __builtin_amdgcn_raw_buffer_store_b128(unit, rs, WS_STAT + (G * 32 + kvh * 4 + (c >> 2)) * 1024 + ((4 * cb + (c & 3)) * 4 + s) * 16, 0, 16);
      }
    }
    STAMP(6);
    if (WOLATE) { lds_wait<2>(cnt + C_VI, 4u); WO_LOAD(); SB(); }
    lds_wait<0>(cnt + C_OA, 4u);
    STAMP(7);
#pragma unroll
    for (int hf = 0; hf < 2; ++hf) {
      const int pos = (lane >> 5) + 2 * hf, row = 4 * w + pos, j = lane & 31;
      f32x4 o = {0.f, 0.f, 0.f, 0.f};
#pragma unroll
      for (int aa = 0; aa < 4; ++aa) o += *(const LAS f32x4*)(L + L_K + (aa + 4 * (row >> 3)) * 4096 + (row & 7) * 512 + ((j ^ (row & 7)) << 4));
      const v4u unit = {cvtpk(o[0], o[1]), tag, cvtpk(o[2], o[3]), tag};
      __builtin_amdgcn_raw_buffer_store_b128(unit, rs, WS_O + (G * 32 + kvh * 4 + w) * 32768 + (4 * cb + pos) * 2048 + s * 512 + j * 16, 0, 16);
    }
    __builtin_amdgcn_raw_buffer_store_b32(tag, rs, lane == 0 ? WS_F1 + (G * 32 + kvh * 4 + w) * 2048 + (cb * 4 + s) * 128 : OOB, 0, 16);
    SB();
    STAMP(8);
    if (WOLATE) WO_REGROUP();
    STAMP(3);
    {
      wait_hints<(APOLL & 1) != 0>(rs, lane < 4 ? WS_F1 + (G * 32 + h) * 2048 + (w * 4 + lane) * 128 : OOB, tag);
      STAMP(9);
      const int so = WS_STAT + (G * 32 + h) * 1024 + (w * 16 + (lane & 15)) * 16;
      const int oo = WS_O + (G * 32 + h) * 32768 + (4 * w + (lane >> 5)) * 2048 + (lane & 31) * 16;
      v4u t[8];
      float wv;
      _Pragma("nounroll") for (unsigned spins = 0; spins < SPIN_CAP; ++spins) {
        CB();
        const v4u st4 = __builtin_amdgcn_raw_buffer_load_b128(rs, so, 0, 16);
#pragma unroll
        for (int i = 0; i < 8; ++i) t[i] = __builtin_amdgcn_raw_buffer_load_b128(rs, oo + (i >> 2) * 4096 + (i & 3) * 512, 0, 16);
        SB();
        const float mo = __uint_as_float(st4[0]), lo = __uint_as_float(st4[2]);
        float M = fmaxf(mo, dppf<0xB1>(mo)); M = fmaxf(M, dppf<0x4E>(M));
        const float e = __builtin_amdgcn_exp2f(mo - M);
        float Lt = e * lo; Lt += dppf<0xB1>(Lt); Lt += dppf<0x4E>(Lt);
        wv = e * __builtin_amdgcn_rcpf(Lt);
        unsigned bad = (st4[1] ^ tag) | (st4[3] ^ tag);
#pragma unroll
        for (int i = 0; i < 8; ++i) bad |= (t[i][1] ^ tag) | (t[i][3] ^ tag);
        if (!__any(bad != 0u)) break;
      }
      STAMP(10);
#pragma unroll
      for (int hf = 0; hf < 2; ++hf) {
        float a0 = 0.f, a1 = 0.f, a2 = 0.f, a3 = 0.f;
#pragma unroll
        for (int sr = 0; sr < 4; ++sr) {
          const float w0 = __int_as_float(__builtin_amdgcn_readlane(__float_as_int(wv), 8 * hf + sr)), w1 = __int_as_float(__builtin_amdgcn_readlane(__float_as_int(wv), 8 * hf + 4 + sr));
          const float wg = lane < 32 ? w0 : w1;
          const v4u uu = t[4 * hf + sr];
          a0 = fmaf(wg, bflo(uu[0]), a0); a1 = fmaf(wg, bfhi(uu[0]), a1);
          a2 = fmaf(wg, bflo(uu[2]), a2); a3 = fmaf(wg, bfhi(uu[2]), a3);
        }
        *(LAS v2u*)(L + L_AI + (4 * w + (lane >> 5) + 2 * hf) * 272 + (lane & 31) * 8) = v2u{cvtpk(a0, a1), cvtpk(a2, a3)};
      }
    }
    lds_signal(cnt + C_AI);
    lds_wait<0>(cnt + C_AI, 4u);
    if (H2MODE == 0) __builtin_amdgcn_raw_buffer_store_b32(tag, rs, lane == 0 ? WS_F2 + (((G * 4 + cb) * 4 + w) * 32 + h) * 128 : OOB, 0, 16);
    STAMP(11);
    v4u af[4];
#pragma unroll
    for (int u2 = 0; u2 < 4; ++u2) af[u2] = *(const LAS v4u*)(L + L_AI + c * 272 + u2 * 64 + g * 16);
#pragma unroll
    for (int mm = 0; mm < 2; ++mm) {
      f32x4 y = {0.f, 0.f, 0.f, 0.f};
#pragma unroll
      for (int u2 = 0; u2 < 4; ++u2) y = mfma(af[u2], bw[mm][u2], y);
      const v4u unit = {cvtpk(y[0], y[1]), tag, cvtpk(y[2], y[3]), tag};
      __builtin_amdgcn_raw_buffer_store_b128(unit, rs, WS_Y + ((G * 4 + g) * 32 + 8 * cb + 2 * w + mm) * 8192 + (h * 16 + c) * 16, 0, 16);
    }
    if (H2MODE == 1) lds_signal(cnt + C_Y);
    STAMP(12);
  } else {
    const int a = w - 4;
    const uint16_t* vp = vc + kvbase + a * 2048 + g * 128 + 8 * c;
    v4u vf[32];
    _Pragma("nounroll") for (int t = PRET; t < 8; ++t) {
      if (t == 7) lds_wait<1>(cnt + C_QR, 4u);
#pragma unroll
      for (int i = 0; i < 4; ++i) dma16(kp + t * 16384 + i * 1024 + ((c ^ (4 * g + i)) << 4), lb + t * 16384 + i * 1024);
    }
    lds_signal(cnt + C_KI);
    STAMP(2);
#pragma unroll
    for (int i = 0; i < KSIG; ++i) vf[i] = ldnt(vp + (i >> 2) * 8192 + (i & 3) * 512);
    SB();
    waitvm<KSIG>();
    lds_signal(cnt + C_KW + a);
    SB();
    STAMP(4);
#pragma unroll
    for (int i = KSIG; i < 32; ++i) vf[i] = ldnt(vp + (i >> 2) * 8192 + (i & 3) * 512);
    SB();
    lds_signal(cnt + C_VI);
    STAMP(3);
    const unsigned tag = __builtin_amdgcn_readfirstlane(ep) + 1u;
    lds_wait<2>(cnt + C_PW + a, 1u);
    STAMP(5);
    v4u pf[4];
#pragma unroll
    for (int jj = 0; jj < 4; ++jj) pf[jj] = *(const LAS v4u*)(L + L_P + (a * 4 + jj) * 1024 + lane * 16);
    f32x4 acc[8];
#pragma unroll
    for (int j = 0; j < 8; ++j) acc[j] = f32x4{0.f, 0.f, 0.f, 0.f};
#pragma unroll
    for (int jj = 0; jj < 4; ++jj) {
      v4u fr[8];
      transpose8(vf + 8 * jj, fr);
#pragma unroll
      for (int j = 0; j < 8; ++j) acc[j] = mfma(fr[j], pf[jj], acc[j]);
    }
    STAMP(6);
    {
      LAS unsigned char* p = L + L_K + (a + 4 * (c >> 3)) * 4096 + (c & 7) * 512;
#pragma unroll
      for (int j = 0; j < 8; ++j) *(LAS f32x4*)(p + (((8 * g + j) ^ (c & 7)) << 4)) = acc[j];
    }
    lds_signal(cnt + C_OA);
    STAMP(7);
    if (a == 0) {
      v2u u[8];
      const int yo = WS_Y + (b * 32 + h) * 8192 + g * 2048 + c * 16;
      if (H2MODE == 0) {
        wait_hints<(APOLL & 2) != 0>(rs, lane < 32 ? WS_F2 + (((G * 4 + (h >> 3)) * 4 + ((h & 7) >> 1)) * 32 + lane) * 128 : OOB, tag);
        STAMP(11);
        _Pragma("nounroll") for (unsigned spins = 0; spins < SPIN_CAP; ++spins) {
          CB();
          v4u t[8];
#pragma unroll
          for (int i = 0; i < 8; ++i) t[i] = __builtin_amdgcn_raw_buffer_load_b128(rs, yo + i * 256, 0, 16);
          unsigned bad = 0;
#pragma unroll
          for (int i = 0; i < 8; ++i) { bad |= (t[i][1] ^ tag) | (t[i][3] ^ tag); u[i] = v2u{t[i][0], t[i][2]}; }
          if (!__any(bad != 0u)) break;
        }
      } else {
        lds_wait<2>(cnt + (H2MODE == 1 ? C_Y : C_AI), 4u);
        STAMP(11);
        int o[8];
#pragma unroll
        for (int i = 0; i < 8; ++i) { o[i] = yo + i * 256; u[i] = v2u{0u, 0u}; }
        poll_units<8, 0>(rs, o, tag, u);
      }
      STAMP(12);
      float y0 = 0.f, y1 = 0.f, y2 = 0.f, y3 = 0.f;
#pragma unroll
      for (int i = 0; i < 8; ++i) { y0 += bflo(u[i][0]); y1 += bfhi(u[i][0]); y2 += bflo(u[i][1]); y3 += bfhi(u[i][1]); }
      const auto s02 = __builtin_amdgcn_permlane32_swap(__float_as_uint(y0), __float_as_uint(y2), false, false);
      const auto s13 = __builtin_amdgcn_permlane32_swap(__float_as_uint(y1), __float_as_uint(y3), false, false);
      const float z0 = __uint_as_float(s02[0]) + __uint_as_float(s02[1]), z1 = __uint_as_float(s13[0]) + __uint_as_float(s13[1]);
      const auto s01 = __builtin_amdgcn_permlane16_swap(__float_as_uint(z0), __float_as_uint(z1), false, false);
      const float y = __uint_as_float(s01[0]) + __uint_as_float(s01[1]);
      *(GAS uint16_t*)(out + (size_t)(b * 4 + g) * 512 + 16 * h + c) = (uint16_t)cvtpk(y, y);
      __builtin_amdgcn_raw_buffer_store_b32(tag, rs, (cb == 0 && h == 0 && lane == 0) ? WS_EPOCH + G * 256 : OOB, 0, 16);
      STAMP(13);
    }
  }
  asm volatile("" :: "v"(cpf));
}

extern "C" void kernel_launch(void* const* d_in, const int* in_sizes, int n_in, void* d_out, int out_size, void* d_ws, size_t ws_size,
                              hipStream_t stream) {
  (void)hipFuncSetAttribute((const void*)gqa_fused, hipFuncAttributeMaxDynamicSharedMemorySize, L_END);
  gqa_fused<<<256, 512, L_END, stream>>>((const uint16_t*)d_in[0], (const uint16_t*)d_in[1], (const uint16_t*)d_in[2], (const uint16_t*)d_in[3],
                                         (const uint16_t*)d_in[4], (uint16_t*)d_out, (unsigned char*)d_ws);
}
```

```cpp
#include <hip/hip_runtime.h>
#include <stdint.h>

#define LAS __attribute__((address_space(3)))
#define GAS __attribute__((address_space(1)))
typedef unsigned v4u __attribute__((ext_vector_type(4)));
typedef unsigned v2u __attribute__((ext_vector_type(2)));
typedef float f32x4 __attribute__((ext_vector_type(4)));
typedef short bf16x8 __attribute__((ext_vector_type(8)));

#define KSIG 8
#define WOLATE 1
#define APOLL 0
#define H2MODE 0
#define CPFN 24
#define H1MODE 1
#define WOAT 32
#define RGFIRST 0
#define WOPOS 0
#define D1S 8
#define D2S 8
#define PRET 1
#define CODEPF 1

constexpr int WS_EPOCH = 0;
constexpr int WS_Q = 4096;
constexpr int WS_STAT = WS_Q + 64 * 16384;
constexpr int WS_F1 = WS_STAT + 64 * 1024;
constexpr int WS_F2 = WS_F1 + 64 * 2048;
constexpr int WS_O = WS_F2 + 32 * 4096;
constexpr int WS_Y = WS_O + 64 * 16384;
constexpr int WS_END = WS_Y + 256 * 4096;
constexpr unsigned EMPTY = 0xffffffffu;
constexpr int WS_STAMP = WS_END;
constexpr int OOB = (int)0x80000000;
constexpr int L_K = 0;
constexpr int L_W = 32768;
constexpr int L_QP = 114688;
constexpr int L_P = 131072;
constexpr int L_QI = L_P + 16384;
constexpr int L_AI = L_QI + 4352;
constexpr int L_MAX = L_AI + 4352;
constexpr int L_SUM = L_MAX + 256;
constexpr int L_CNT = L_SUM + 256;
constexpr int L_END = L_CNT + 128;
enum { C_QP = 0, C_QI = 1, C_MAX = 2, C_P = 3, C_PW = 4  , C_KW = 8  , C_KI = 12, C_OA = 13, C_AI = 14, C_QR = 15, C_VI = 16, C_W = 17, C_Y = 18 };
constexpr unsigned SPIN_CAP = 1u << 18;

#define SB() __builtin_amdgcn_sched_barrier(0)
#define CB() asm volatile("" ::: "memory")

__device__ __forceinline__ f32x4 mfma(v4u a, v4u b, f32x4 c) {
  return __builtin_amdgcn_mfma_f32_16x16x32_bf16(__builtin_bit_cast(bf16x8, a), __builtin_bit_cast(bf16x8, b), c, 0, 0, 0);
}
__device__ __forceinline__ unsigned cvtpk(float lo, float hi) {
  typedef float f32x2_ __attribute__((ext_vector_type(2)));
  typedef __bf16 bf16x2_ __attribute__((ext_vector_type(2)));
  return __builtin_bit_cast(unsigned, __builtin_convertvector(f32x2_{lo, hi}, bf16x2_));
}
__device__ __forceinline__ void transpose8(const v4u* in, v4u* out) {
#pragma unroll
  for (int w = 0; w < 4; ++w) {
#pragma unroll
    for (int u = 0; u < 4; ++u) {
      out[2 * w][u] = __builtin_amdgcn_perm(in[2 * u + 1][w], in[2 * u][w], 0x05040100u);
      out[2 * w + 1][u] = __builtin_amdgcn_perm(in[2 * u + 1][w], in[2 * u][w], 0x07060302u);
    }
  }
}
__device__ __forceinline__ v4u ldnt(const void* p) { return __builtin_nontemporal_load((const GAS v4u*)p); }
#define WPOL 1
__device__ __forceinline__ v4u ldw(const void* p) { return WPOL ? *(const GAS v4u*)p : __builtin_nontemporal_load((const GAS v4u*)p); }
__device__ __forceinline__ float xor16(float x, bool mx) {
  auto s = __builtin_amdgcn_permlane16_swap(__float_as_uint(x), __float_as_uint(x), false, false);
  return mx ? fmaxf(__uint_as_float(s[0]), __uint_as_float(s[1])) : __uint_as_float(s[0]) + __uint_as_float(s[1]);
}
__device__ __forceinline__ float xor32(float x, bool mx) {
  auto s = __builtin_amdgcn_permlane32_swap(__float_as_uint(x), __float_as_uint(x), false, false);
  return mx ? fmaxf(__uint_as_float(s[0]), __uint_as_float(s[1])) : __uint_as_float(s[0]) + __uint_as_float(s[1]);
}
template <int CTRL> __device__ __forceinline__ float dppf(float x) {
  return __builtin_bit_cast(float, __builtin_amdgcn_mov_dpp(__builtin_bit_cast(int, x), CTRL, 0xf, 0xf, true));
}
__device__ __forceinline__ float bflo(unsigned u) { return __uint_as_float(u << 16); }
__device__ __forceinline__ float bfhi(unsigned u) { return __uint_as_float(u & 0xffff0000u); }

__device__ __forceinline__ void lds_signal(LAS unsigned* cnt) {
  const unsigned addr = (unsigned)(uintptr_t)cnt, one = 1u;
  asm volatile("s_mov_b64 exec, 1\n\tds_add_u32 %0, %1\n\ts_mov_b64 exec, -1" :: "v"(addr), "v"(one) : "memory");
}
template <int SLEEP>
__device__ __forceinline__ void lds_wait(LAS unsigned* cnt, unsigned target) {
  CB();
  _Pragma("nounroll") for (unsigned spins = 0; spins < (SPIN_CAP << 4); ++spins) {
    const unsigned v = __builtin_amdgcn_readfirstlane(__hip_atomic_load(cnt, __ATOMIC_RELAXED, __HIP_MEMORY_SCOPE_WORKGROUP));
    if (v >= target) break;
    if (SLEEP) __builtin_amdgcn_s_sleep(SLEEP);
  }
  CB();
}
#define RAWBAR() do { SB(); asm volatile("s_waitcnt lgkmcnt(0)" ::: "memory"); __builtin_amdgcn_s_barrier(); CB(); SB(); } while (0)

__device__ __forceinline__ void dma16(const void* gsrc, unsigned lds_dst) {
  unsigned keep;
  asm volatile("s_mov_b32 %0, m0\n\ts_mov_b32 m0, %2\n\ts_nop 0\n\tglobal_load_lds_dwordx4 %1, off nt\n\ts_mov_b32 m0, %0" : "=&s"(keep) : "v"(gsrc), "s"(lds_dst) : "memory");
}
template <int N> __device__ __forceinline__ void waitvm() { asm volatile("s_waitcnt vmcnt(%0)" :: "i"(N) : "memory"); }
template <int N, int SLEEP>
__device__ __forceinline__ void poll_units(__amdgpu_buffer_rsrc_t rs, int (&o)[N], unsigned tag, v2u (&u)[N]) {
  _Pragma("nounroll") for (unsigned spins = 0; spins < SPIN_CAP; ++spins) {
    CB();
    v4u t[N];
#pragma unroll
    for (int i = 0; i < N; ++i) t[i] = __builtin_amdgcn_raw_buffer_load_b128(rs, o[i], 0, 16);
    bool pend = false;
#pragma unroll
    for (int i = 0; i < N; ++i) {
      const bool was = o[i] >= 0;
      const bool bad = was & ((t[i][1] != tag) | (t[i][3] != tag));
      u[i][0] = was ? t[i][0] : u[i][0];
      u[i][1] = was ? t[i][2] : u[i][1];
      o[i] = bad ? o[i] : OOB;
      pend |= bad;
    }
    if (!__any(pend)) break;
    if (SLEEP) __builtin_amdgcn_s_sleep(SLEEP);
  }
}
__device__ __forceinline__ void wait_hints(__amdgpu_buffer_rsrc_t rs, int fo, unsigned tag) {
  _Pragma("nounroll") for (unsigned spins = 0; spins < SPIN_CAP; ++spins) {
    CB();
    const v2u f = __builtin_amdgcn_raw_buffer_load_b64(rs, fo, 0, 16);
    fo = ((f[0] == tag) & (f[1] == tag)) ? OOB : fo;
    if (!__any(fo >= 0)) break;
  }
  CB();
}

#define STAMP(i) do {} while (0)

__global__ void __launch_bounds__(512, 2) gqa_fused(const uint16_t* __restrict__ x, const uint16_t* __restrict__ kc, const uint16_t* __restrict__ vc,
                                                     const uint16_t* __restrict__ wq, const uint16_t* __restrict__ wo, uint16_t* out, unsigned char* ws) {
  extern __shared__ __attribute__((aligned(16))) unsigned char smem[];
  LAS unsigned char* L = (LAS unsigned char*)smem;
  LAS unsigned* cnt = (LAS unsigned*)(L + L_CNT);
  STAMP(0);
  const int tid = threadIdx.x, lane = tid & 63, w = __builtin_amdgcn_readfirstlane(tid >> 6), g = lane >> 4, c = lane & 15;
  const int bid = blockIdx.x, kvh = bid & 7, s = (bid >> 3) & 3, b = bid >> 5, G = b >> 2, cb = b & 3;
  unsigned cpf = 0;
  if (w == 7 && lane < 4 && (bid >> 3) < CPFN) cpf = *(const GAS unsigned*)((__builtin_amdgcn_s_getpc() & ~127ull) + (size_t)((bid >> 3) * 4 + lane) * 128);
  const __amdgpu_buffer_rsrc_t rs = __builtin_amdgcn_make_buffer_rsrc((void*)ws, 0, WS_END, 0x00020000);
  const size_t kvbase = ((size_t)(b * 8 + kvh) * 2048 + s * 512) * 128;
  const int h = kvh * 4 + s;
  constexpr float C2 = 0.088388347648318440f * 1.4426950408889634f;
  const unsigned ep = __builtin_amdgcn_raw_buffer_load_b32(rs, WS_EPOCH + G * 256, 0, 16);

  const int hh = (cb * 4 + s) & 7, kh = cb >> 1;
  v4u xf[2], wr[8];
  const unsigned char* kp = (const unsigned char*)(kc + kvbase) + (w & 3) * 4096 + g * 256;
  const unsigned lb = (unsigned)(uintptr_t)(L + L_K) + (w & 3) * 4096;
  if (w < 4) {
    const int k0 = 256 * kh + 64 * w + 32 * (c >> 3) + 8 * g;
    const uint16_t* p = wq + (size_t)k0 * 4096 + kvh * 512 + hh * 64 + 8 * (c & 7);
#pragma unroll
    for (int i = 0; i < 8; ++i) wr[i] = ldw(p + (size_t)i * 4096);
#pragma unroll
    for (int e = 0; e < 2; ++e) xf[e] = *(const GAS v4u*)(x + (size_t)(16 * G + 8 * e + (c & 7)) * 512 + k0);
    if (tid < 32) cnt[tid] = 0u;
  } else {
#pragma unroll
    for (int t = 0; t < PRET; ++t)
#pragma unroll
      for (int i = 0; i < 4; ++i) dma16(kp + t * 16384 + i * 1024 + ((c ^ (4 * g + i)) << 4), lb + t * 16384 + i * 1024);
  }
  RAWBAR();

  if (w < 4) {
    v4u wf[8];
    SB();
    {
      v4u fr[8];
      transpose8(wr, fr);
      LAS unsigned char* p = L + L_QP + ((w * 2 + (g >> 1)) * 16 + 4 * (g & 1)) * 256 + (c & 7) * 32;
#pragma unroll
      for (int e = 0; e < 2; ++e) {
        f32x4 acc[8];
#pragma unroll
        for (int j = 0; j < 8; ++j) acc[j] = mfma(xf[e], fr[j], f32x4{0.f, 0.f, 0.f, 0.f});
        if ((g >> 1) == (c >> 3)) {
#pragma unroll
          for (int r = 0; r < 4; ++r) {
            *(LAS f32x4*)(p + (8 * e + r) * 256) = f32x4{acc[0][r], acc[1][r], acc[2][r], acc[3][r]};
            *(LAS f32x4*)(p + (8 * e + r) * 256 + 16) = f32x4{acc[4][r], acc[5][r], acc[6][r], acc[7][r]};
          }
        }
      }
    }
    lds_signal(cnt + C_QP);
    STAMP(1);
    const unsigned tag = __builtin_amdgcn_readfirstlane(ep) + 1u;
    lds_wait<0>(cnt + C_QP, 4u);
    {
      const int r = tid >> 4, nq = tid & 15;
      f32x4 sum = {0.f, 0.f, 0.f, 0.f};
#pragma unroll
      for (int p = 0; p < 8; ++p) sum += *(const LAS f32x4*)(L + L_QP + (p * 16 + r) * 256 + nq * 16);
      lds_signal(cnt + C_QR);
      const v4u unit = {cvtpk(sum[0], sum[1]), tag, cvtpk(sum[2], sum[3]), tag};
      const int row = (hh >> 1) * 4 + (r & 3), dq = (hh & 1) * 16 + nq;
      __builtin_amdgcn_raw_buffer_store_b128(unit, rs, WS_Q + (((4 * G + w) * 8 + kvh) * 2 + kh) * 8192 + (row * 32 + dq) * 16, 0, 16);
    }
    const int opub = WS_O + (G * 32 + kvh * 4 + w) * 16384 + ((4 * cb + g) * 4 + s) * 256 + c * 16;
    const int ypub = WS_Y + ((G * 4 + g) * 32 + 8 * cb + 2 * w + (c & 1)) * 4096 + h * 128 + (c >> 1) * 16;
    __builtin_amdgcn_raw_buffer_store_b128(v4u{EMPTY, EMPTY, EMPTY, EMPTY}, rs, opub, 0, 16);
    __builtin_amdgcn_raw_buffer_store_b128(v4u{EMPTY, EMPTY, EMPTY, EMPTY}, rs, ypub, 0, 16);
    STAMP(2);
    const uint16_t* wop = wo + (size_t)(h * 128 + 32 * w + 2 * g) * 512 + 128 * cb + 8 * c;
#define WO_LOAD() _Pragma("unroll") for (int i = 0; i < 8; ++i) wf[i] = ldw(wop + (size_t)(8 * (i & 3) + (i >> 2)) * 512)
    if (!WOLATE) { WO_LOAD(); }
    SB();
    lds_wait<2>(cnt + C_KI, 4u);
    {
      int o[4]; v2u u[4];
#pragma unroll
      for (int i = 0; i < 4; ++i) o[i] = WS_Q + (b * 8 + kvh) * 16384 + i * 4096 + tid * 16;
      poll_units<4, 2>(rs, o, tag, u);
#pragma unroll
      for (int i = 0; i < 2; ++i)
        *(LAS v2u*)(L + L_QI + ((tid >> 5) + 8 * i) * 272 + (tid & 31) * 8) =
            v2u{cvtpk(bflo(u[i][0]) + bflo(u[i + 2][0]), bfhi(u[i][0]) + bfhi(u[i + 2][0])), cvtpk(bflo(u[i][1]) + bflo(u[i + 2][1]), bfhi(u[i][1]) + bfhi(u[i + 2][1]))};
    }
    lds_signal(cnt + C_QI);
    waitvm<0>();
    STAMP(4);
    lds_wait<1>(cnt + C_KW + w, 1u);
    STAMP(14);
    v4u kf[32];
    {
      const int krow = 4 * (c & 3) + (c >> 2);
#pragma unroll
      for (int t = 0; t < 8; ++t)
#pragma unroll
        for (int stp = 0; stp < 4; ++stp) kf[t * 4 + stp] = *(const LAS v4u*)(L + L_K + (w + 4 * t) * 4096 + krow * 256 + (((4 * stp + g) ^ c) << 4));
    }
    SB();
    lds_wait<0>(cnt + C_QI, 4u);
    v4u qf[4];
#pragma unroll
    for (int stp = 0; stp < 4; ++stp) qf[stp] = *(const LAS v4u*)(L + L_QI + c * 272 + stp * 64 + g * 16);
    f32x4 sc[8];
#pragma unroll
    for (int t = 0; t < 8; ++t) sc[t] = f32x4{0.f, 0.f, 0.f, 0.f};
#pragma unroll
    for (int stp = 0; stp < 4; ++stp)
#pragma unroll
      for (int t = 0; t < 8; ++t) sc[t] = mfma(kf[t * 4 + stp], qf[stp], sc[t]);
    float m = sc[0][0];
#pragma unroll
    for (int t = 0; t < 8; ++t)
#pragma unroll
      for (int r = 0; r < 4; ++r) m = fmaxf(m, sc[t][r]);
    m = xor32(xor16(m, true), true);
    if (g == 0) *(LAS float*)(L + L_MAX + (w * 16 + c) * 4) = m;
    lds_signal(cnt + C_MAX);
    STAMP(15);
    lds_wait<0>(cnt + C_MAX, 4u);
#pragma unroll
    for (int ww = 0; ww < 4; ++ww) m = fmaxf(m, *(const LAS float*)(L + L_MAX + (ww * 16 + c) * 4));
    const float ms = m * C2;
    float l0 = 0.f, l1 = 0.f;
#pragma unroll
    for (int t = 0; t < 8; ++t) {
#pragma unroll
      for (int r = 0; r < 4; ++r) sc[t][r] = __builtin_amdgcn_exp2f(fmaf(sc[t][r], C2, -ms));
      l0 += sc[t][0] + sc[t][1]; l1 += sc[t][2] + sc[t][3];
    }
#pragma unroll
    for (int jj = 0; jj < 4; ++jj) {
      const v4u pf = {cvtpk(sc[2 * jj][0], sc[2 * jj][1]), cvtpk(sc[2 * jj][2], sc[2 * jj][3]), cvtpk(sc[2 * jj + 1][0], sc[2 * jj + 1][1]), cvtpk(sc[2 * jj + 1][2], sc[2 * jj + 1][3])};
      *(LAS v4u*)(L + L_P + (w * 4 + jj) * 1024 + lane * 16) = pf;
    }
    lds_signal(cnt + C_PW + w);
    const float l = xor32(xor16(l0 + l1, false), false);
    if (g == 0) *(LAS float*)(L + L_SUM + (w * 16 + c) * 4) = l;
    lds_signal(cnt + C_P);
    STAMP(5);
    v4u bw[2][4];
#define WO_REGROUP() do { v4u wt[8]; transpose8(wf, wt); \
      _Pragma("unroll") for (int j = 0; j < 8; ++j) *(LAS v4u*)(L + L_W + (j * 4 + w) * 1056 + lane * 16) = wt[j]; \
      lds_signal(cnt + C_W); lds_wait<0>(cnt + C_W, 4u); \
      _Pragma("unroll") for (int mm = 0; mm < 2; ++mm) _Pragma("unroll") for (int u = 0; u < 4; ++u) \
        bw[mm][u] = *(const LAS v4u*)(L + L_W + ((c & 7) * 4 + u) * 1056 + (g * 16 + 2 * (2 * w + mm) + (c >> 3)) * 16); } while (0)
    if (!WOLATE) WO_REGROUP();
    if (w == 0) {
      lds_wait<0>(cnt + C_P, 4u);
      float L4 = 0.f;
#pragma unroll
      for (int ww = 0; ww < 4; ++ww) L4 += *(const LAS float*)(L + L_SUM + (ww * 16 + c) * 4);
      __builtin_amdgcn_raw_buffer_store_b64(v2u{tag, tag}, rs, lane == 0 ? WS_F2 + ((G * 4 + cb) * 32 + h) * 128 : OOB, 0, 16);
      if (g == 0) {
        const v4u unit = {__float_as_uint(ms), tag, __float_as_uint(L4), tag};
        __builtin_amdgcn_raw_buffer_store_b128(unit, rs, WS_STAT + (G * 32 + kvh * 4 + (c >> 2)) * 1024 + ((4 * cb + (c & 3)) * 4 + s) * 16, 0, 16);
      }
    }
    STAMP(6);
    if (WOLATE) { lds_wait<2>(cnt + C_VI, 4u); if (WOPOS == 0) { WO_LOAD(); } SB(); }
    v4u stv = {0u, 0u, 0u, 0u};
    if (H1MODE) { stv = __builtin_amdgcn_raw_buffer_load_b128(rs, WS_STAT + (G * 32 + h) * 1024 + ((4 * w + g) * 4 + (c & 3)) * 16, 0, 16); SB(); }
    float wv;
#define WEIGHTS() do { int o1[1] = {((stv[1] != tag) | (stv[3] != tag)) ? WS_STAT + (G * 32 + h) * 1024 + ((4 * w + g) * 4 + (c & 3)) * 16 : OOB}; v2u u1[1] = {v2u{stv[0], stv[2]}}; \
      if (__any(o1[0] >= 0)) poll_units<1, 0>(rs, o1, tag, u1); \
      const float mo = __uint_as_float(u1[0][0]), lo = __uint_as_float(u1[0][1]); \
      float M = fmaxf(mo, dppf<0xB1>(mo)); M = fmaxf(M, dppf<0x4E>(M)); \
      const float e = __builtin_amdgcn_exp2f(mo - M); \
      float Lt = e * lo; Lt += dppf<0xB1>(Lt); Lt += dppf<0x4E>(Lt); \
      wv = e * __builtin_amdgcn_rcpf(Lt); } while (0)
    if (RGFIRST) { WO_REGROUP(); }
    STAMP(3);
    if (WOPOS == 1) { lds_wait<0>(cnt + C_OA, 1u); WO_LOAD(); SB(); }
    lds_wait<0>(cnt + C_OA, 4u);
    STAMP(7);
    {
      const int row = 4 * w + g;
      f32x4 o0 = {0.f, 0.f, 0.f, 0.f}, o1 = {0.f, 0.f, 0.f, 0.f};
#pragma unroll
      for (int aa = 0; aa < 4; ++aa) {
        const LAS unsigned char* p = L + L_K + (aa + 4 * (row >> 3)) * 4096 + (row & 7) * 512;
        o0 += *(const LAS f32x4*)(p + (((2 * c) ^ (row & 7)) << 4));
        o1 += *(const LAS f32x4*)(p + (((2 * c + 1) ^ (row & 7)) << 4));
      }
      const v4u pk = {min(cvtpk(o0[0], o0[1]), EMPTY - 1u), min(cvtpk(o0[2], o0[3]), EMPTY - 1u), min(cvtpk(o1[0], o1[1]), EMPTY - 1u), min(cvtpk(o1[2], o1[3]), EMPTY - 1u)};
      __builtin_amdgcn_raw_buffer_store_b128(pk, rs, opub, 0, 16);
    }
    if (H1MODE == 0) __builtin_amdgcn_raw_buffer_store_b64(v2u{tag, tag}, rs, lane == 0 ? WS_F1 + (G * 32 + kvh * 4 + w) * 2048 + (cb * 4 + s) * 128 : OOB, 0, 16);
    SB();
    STAMP(8);
    if (WOPOS == 2) { WO_LOAD(); SB(); }
    if (WOLATE && !RGFIRST) WO_REGROUP();
    if (RGFIRST) WEIGHTS();
    {
      const int so = WS_STAT + (G * 32 + h) * 1024 + ((4 * w + g) * 4 + (c & 3)) * 16;
      const int oo = WS_O + (G * 32 + h) * 16384 + (4 * w + g) * 1024 + c * 16;
      v4u t[4];
      if (H1MODE == 0) {
        wait_hints(rs, lane < 4 ? WS_F1 + (G * 32 + h) * 2048 + (w * 4 + lane) * 128 : OOB, tag);
        STAMP(9);
        _Pragma("nounroll") for (unsigned spins = 0; spins < SPIN_CAP; ++spins) {
          CB();
          const v4u st4 = __builtin_amdgcn_raw_buffer_load_b128(rs, so, 0, 16);
#pragma unroll
          for (int i = 0; i < 4; ++i) t[i] = __builtin_amdgcn_raw_buffer_load_b128(rs, oo + i * 256, 0, 16);
          SB();
          const float mo = __uint_as_float(st4[0]), lo = __uint_as_float(st4[2]);
          float M = fmaxf(mo, dppf<0xB1>(mo)); M = fmaxf(M, dppf<0x4E>(M));
          const float e = __builtin_amdgcn_exp2f(mo - M);
          float Lt = e * lo; Lt += dppf<0xB1>(Lt); Lt += dppf<0x4E>(Lt);
          wv = e * __builtin_amdgcn_rcpf(Lt);
          unsigned mx = 0;
#pragma unroll
          for (int i = 0; i < 4; ++i) mx = max(max(mx, max(t[i][0], t[i][1])), max(t[i][2], t[i][3]));
          if (!__any((mx == EMPTY) | (st4[1] != tag) | (st4[3] != tag))) break;
        }
      } else {
        if (!RGFIRST) WEIGHTS();
        if (D1S) __builtin_amdgcn_s_sleep(D1S);
        STAMP(9);
        _Pragma("nounroll") for (unsigned spins = 0; spins < SPIN_CAP; ++spins) {
          CB();
#pragma unroll
          for (int i = 0; i < 4; ++i) t[i] = __builtin_amdgcn_raw_buffer_load_b128(rs, oo + i * 256, 0, 16);
          unsigned mx = 0;
#pragma unroll
          for (int i = 0; i < 4; ++i) mx = max(max(mx, max(t[i][0], t[i][1])), max(t[i][2], t[i][3]));
          if (!__any(mx == EMPTY)) break;
          if (H1MODE > 1) __builtin_amdgcn_s_sleep(H1MODE);
        }
      }
      STAMP(10);
      const float wg[4] = {dppf<0x00>(wv), dppf<0x55>(wv), dppf<0xAA>(wv), dppf<0xFF>(wv)};
      v4u o4;
#pragma unroll
      for (int k = 0; k < 4; ++k) {
        float a0 = 0.f, a1 = 0.f;
#pragma unroll
        for (int sr = 0; sr < 4; ++sr) { a0 = fmaf(wg[sr], bflo(t[sr][k]), a0); a1 = fmaf(wg[sr], bfhi(t[sr][k]), a1); }
        o4[k] = cvtpk(a0, a1);
      }
      *(LAS v4u*)(L + L_AI + (4 * w + g) * 272 + c * 16) = o4;
    }
    lds_signal(cnt + C_AI);
    lds_wait<0>(cnt + C_AI, 4u);
    STAMP(11);
    v4u af[4];
#pragma unroll
    for (int u2 = 0; u2 < 4; ++u2) af[u2] = *(const LAS v4u*)(L + L_AI + c * 272 + u2 * 64 + g * 16);
    {
      f32x4 y0 = {0.f, 0.f, 0.f, 0.f}, y1 = {0.f, 0.f, 0.f, 0.f};
#pragma unroll
      for (int u2 = 0; u2 < 4; ++u2) { y0 = mfma(af[u2], bw[0][u2], y0); y1 = mfma(af[u2], bw[1][u2], y1); }
      const unsigned p00 = min(cvtpk(y0[0], y0[1]), EMPTY - 1u), p01 = min(cvtpk(y0[2], y0[3]), EMPTY - 1u);
      const unsigned p10 = min(cvtpk(y1[0], y1[1]), EMPTY - 1u), p11 = min(cvtpk(y1[2], y1[3]), EMPTY - 1u);
      const bool odd = c & 1;
      const unsigned r0 = __builtin_amdgcn_mov_dpp(odd ? p00 : p10, 0xB1, 0xf, 0xf, true), r1 = __builtin_amdgcn_mov_dpp(odd ? p01 : p11, 0xB1, 0xf, 0xf, true);
      __builtin_amdgcn_raw_buffer_store_b128(odd ? v4u{r0, r1, p10, p11} : v4u{p00, p01, r0, r1}, rs, ypub, 0, 16);
    }
    lds_signal(cnt + C_Y);
    STAMP(12);
  } else {
    const int a = w - 4;
    const uint16_t* vp = vc + kvbase + a * 2048 + g * 128 + 8 * c;
    v4u vf[32];
    _Pragma("nounroll") for (int t = PRET; t < 8; ++t) {
      if (t == 7) lds_wait<1>(cnt + C_QR, 4u);
#pragma unroll
      for (int i = 0; i < 4; ++i) dma16(kp + t * 16384 + i * 1024 + ((c ^ (4 * g + i)) << 4), lb + t * 16384 + i * 1024);
    }
    lds_signal(cnt + C_KI);
    STAMP(2);
#pragma unroll
    for (int i = 0; i < KSIG; ++i) vf[i] = ldnt(vp + (i >> 2) * 8192 + (i & 3) * 512);
    SB();
    waitvm<KSIG>();
    lds_signal(cnt + C_KW + a);
    SB();
    STAMP(4);
#pragma unroll
    for (int i = KSIG; i < WOAT; ++i) vf[i] = ldnt(vp + (i >> 2) * 8192 + (i & 3) * 512);
    SB();
    lds_signal(cnt + C_VI);
    SB();
#pragma unroll
    for (int i = WOAT; i < 32; ++i) vf[i] = ldnt(vp + (i >> 2) * 8192 + (i & 3) * 512);
    SB();
    STAMP(3);
    const unsigned tag = __builtin_amdgcn_readfirstlane(ep) + 1u;
    lds_wait<2>(cnt + C_PW + a, 1u);
    STAMP(5);
    v4u pf[4];
#pragma unroll
    for (int jj = 0; jj < 4; ++jj) pf[jj] = *(const LAS v4u*)(L + L_P + (a * 4 + jj) * 1024 + lane * 16);
    f32x4 acc[8];
#pragma unroll
    for (int j = 0; j < 8; ++j) acc[j] = f32x4{0.f, 0.f, 0.f, 0.f};
#pragma unroll
    for (int jj = 0; jj < 4; ++jj) {
      v4u fr[8];
      transpose8(vf + 8 * jj, fr);
#pragma unroll
      for (int j = 0; j < 8; ++j) acc[j] = mfma(fr[j], pf[jj], acc[j]);
    }
    STAMP(6);
    {
      LAS unsigned char* p = L + L_K + (a + 4 * (c >> 3)) * 4096 + (c & 7) * 512;
#pragma unroll
      for (int j = 0; j < 8; ++j) *(LAS f32x4*)(p + (((8 * g + j) ^ (c & 7)) << 4)) = acc[j];
    }
    lds_signal(cnt + C_OA);
    STAMP(7);
    if (a == 0) {
      wait_hints(rs, lane < 32 ? WS_F2 + ((G * 4 + (h >> 3)) * 32 + lane) * 128 : OOB, tag);
      const int yo = WS_Y + (b * 32 + h) * 4096 + lane * 16;
      lds_wait<2>(cnt + C_Y, 4u);
      if (D2S) __builtin_amdgcn_s_sleep(D2S);
      STAMP(11);
      v4u t[4];
      _Pragma("nounroll") for (unsigned spins = 0; spins < SPIN_CAP; ++spins) {
        CB();
#pragma unroll
        for (int i = 0; i < 4; ++i) t[i] = __builtin_amdgcn_raw_buffer_load_b128(rs, yo + i * 1024, 0, 16);
        unsigned mx = 0;
#pragma unroll
        for (int i = 0; i < 4; ++i) mx = max(max(mx, max(t[i][0], t[i][1])), max(t[i][2], t[i][3]));
        if (!__any(mx == EMPTY)) break;
      }
      STAMP(12);
      float v[8];
#pragma unroll
      for (int k = 0; k < 4; ++k) {
        v[2 * k] = (bflo(t[0][k]) + bflo(t[1][k])) + (bflo(t[2][k]) + bflo(t[3][k]));
        v[2 * k + 1] = (bfhi(t[0][k]) + bfhi(t[1][k])) + (bfhi(t[2][k]) + bfhi(t[3][k]));
      }
      float z[4];
#pragma unroll
      for (int k = 0; k < 4; ++k) { const auto sw = __builtin_amdgcn_permlane32_swap(__float_as_uint(v[k]), __float_as_uint(v[k + 4]), false, false); z[k] = __uint_as_float(sw[0]) + __uint_as_float(sw[1]); }
      float q2[2];
#pragma unroll
      for (int k = 0; k < 2; ++k) { const auto sw = __builtin_amdgcn_permlane16_swap(__float_as_uint(z[k]), __float_as_uint(z[k + 2]), false, false); q2[k] = __uint_as_float(sw[0]) + __uint_as_float(sw[1]); }
      const float e0 = q2[0] + dppf<0x128>(q2[0]), e1 = q2[1] + dppf<0x128>(q2[1]);
      const float y = (lane & 8) ? e1 : e0;
      *(GAS uint16_t*)(out + (size_t)(b * 4 + ((lane >> 3) & 3)) * 512 + 16 * h + 2 * (lane & 7) + (lane >> 5)) = (uint16_t)cvtpk(y, y);
      __builtin_amdgcn_raw_buffer_store_b32(tag, rs, (cb == 0 && h == 0 && lane == 0) ? WS_EPOCH + G * 256 : OOB, 0, 16);
      STAMP(13);
    }
  }
  asm volatile("" :: "v"(cpf));
}

extern "C" void kernel_launch(void* const* d_in, const int* in_sizes, int n_in, void* d_out, int out_size, void* d_ws, size_t ws_size,
                              hipStream_t stream) {
  (void)hipFuncSetAttribute((const void*)gqa_fused, hipFuncAttributeMaxDynamicSharedMemorySize, L_END);
  gqa_fused<<<256, 512, L_END, stream>>>((const uint16_t*)d_in[0], (const uint16_t*)d_in[1], (const uint16_t*)d_in[2], (const uint16_t*)d_in[3],
                                         (const uint16_t*)d_in[4], (uint16_t*)d_out, (unsigned char*)d_ws);
}
```

```cpp
#include <hip/hip_runtime.h>
#include <stdint.h>

#define LAS __attribute__((address_space(3)))
#define GAS __attribute__((address_space(1)))
typedef unsigned v4u __attribute__((ext_vector_type(4)));
typedef unsigned v2u __attribute__((ext_vector_type(2)));
typedef float f32x4 __attribute__((ext_vector_type(4)));
typedef short bf16x8 __attribute__((ext_vector_type(8)));
typedef short s16x4 __attribute__((ext_vector_type(4)));

#define D1S 8
#define D2S 8
#define F2G 0
#define STP 0
#define LAG 2
#define WOR 5
#define QJ 3
#define CPFN 24

constexpr int WS_EPOCH = 0;
constexpr int WS_Q = 4096;
constexpr int WS_STAT = WS_Q + 64 * 16384;
constexpr int WS_F2 = WS_STAT + 64 * 1024;
constexpr int WS_O = WS_F2 + 256 * 128;
constexpr int WS_Y = WS_O + 64 * 16384;
constexpr int WS_END = WS_Y + 256 * 4096;
constexpr unsigned EMPTY = 0xffffffffu;
constexpr int WS_STAMP = WS_END;
constexpr int OOB = (int)0x80000000;
constexpr int L_K = 0;
constexpr int L_QP = 114688;
constexpr int L_P = 131072;
constexpr int L_QI = L_P + 16384;
constexpr int L_AI = L_QI + 4352;
constexpr int L_MAX = L_AI + 4352;
constexpr int L_SUM = L_MAX + 256;
constexpr int L_CNT = L_SUM + 256;
constexpr int L_END = L_CNT + 256;
enum { C_QP = 0, C_QI, C_MAX, C_P, C_AI, C_QR, C_W, C_Y, C_PVH, C_R = 16  , C_KC = 32   };
constexpr unsigned SPIN_CAP = 1u << 18;

#define SB() __builtin_amdgcn_sched_barrier(0)
#define CB() asm volatile("" ::: "memory")

__device__ __forceinline__ f32x4 mfma(v4u a, v4u b, f32x4 c) {
  return __builtin_amdgcn_mfma_f32_16x16x32_bf16(__builtin_bit_cast(bf16x8, a), __builtin_bit_cast(bf16x8, b), c, 0, 0, 0);
}
__device__ __forceinline__ unsigned cvtpk(float lo, float hi) {
  typedef float f32x2_ __attribute__((ext_vector_type(2)));
  typedef __bf16 bf16x2_ __attribute__((ext_vector_type(2)));
  return __builtin_bit_cast(unsigned, __builtin_convertvector(f32x2_{lo, hi}, bf16x2_));
}
__device__ __forceinline__ void transpose8(const v4u* in, v4u* out) {
#pragma unroll
  for (int w = 0; w < 4; ++w) {
#pragma unroll
    for (int u = 0; u < 4; ++u) {
      out[2 * w][u] = __builtin_amdgcn_perm(in[2 * u + 1][w], in[2 * u][w], 0x05040100u);
      out[2 * w + 1][u] = __builtin_amdgcn_perm(in[2 * u + 1][w], in[2 * u][w], 0x07060302u);
    }
  }
}
__device__ __forceinline__ v2u trread(LAS unsigned char* p) { return __builtin_bit_cast(v2u, __builtin_amdgcn_ds_read_tr16_b64_v4i16((LAS s16x4*)p)); }
__device__ __forceinline__ float xor16(float x, bool mx) {
  auto s = __builtin_amdgcn_permlane16_swap(__float_as_uint(x), __float_as_uint(x), false, false);
  return mx ? fmaxf(__uint_as_float(s[0]), __uint_as_float(s[1])) : __uint_as_float(s[0]) + __uint_as_float(s[1]);
}
__device__ __forceinline__ float xor32(float x, bool mx) {
  auto s = __builtin_amdgcn_permlane32_swap(__float_as_uint(x), __float_as_uint(x), false, false);
  return mx ? fmaxf(__uint_as_float(s[0]), __uint_as_float(s[1])) : __uint_as_float(s[0]) + __uint_as_float(s[1]);
}
template <int CTRL> __device__ __forceinline__ float dppf(float x) {
  return __builtin_bit_cast(float, __builtin_amdgcn_mov_dpp(__builtin_bit_cast(int, x), CTRL, 0xf, 0xf, true));
}
__device__ __forceinline__ float bflo(unsigned u) { return __uint_as_float(u << 16); }
__device__ __forceinline__ float bfhi(unsigned u) { return __uint_as_float(u & 0xffff0000u); }

__device__ __forceinline__ void lds_signal(LAS unsigned* cnt) {
  const unsigned addr = (unsigned)(uintptr_t)cnt, one = 1u;
  asm volatile("s_mov_b64 exec, 1\n\tds_add_u32 %0, %1\n\ts_mov_b64 exec, -1" :: "v"(addr), "v"(one) : "memory");
}
template <int SLEEP>
__device__ __forceinline__ void lds_wait(LAS unsigned* cnt, unsigned target) {
  CB();
  _Pragma("nounroll") for (unsigned spins = 0; spins < (SPIN_CAP << 4); ++spins) {
    const unsigned v = __builtin_amdgcn_readfirstlane(__hip_atomic_load(cnt, __ATOMIC_RELAXED, __HIP_MEMORY_SCOPE_WORKGROUP));
    if (v >= target) break;
    if (SLEEP) __builtin_amdgcn_s_sleep(SLEEP);
  }
  CB();
}
#define RAWBAR() do { SB(); asm volatile("s_waitcnt lgkmcnt(0)" ::: "memory"); __builtin_amdgcn_s_barrier(); CB(); SB(); } while (0)

__device__ __forceinline__ void dma16(const void* gsrc, unsigned lds_dst) {
  unsigned keep;
  asm volatile("s_mov_b32 %0, m0\n\ts_mov_b32 m0, %2\n\ts_nop 0\n\tglobal_load_lds_dwordx4 %1, off nt\n\ts_mov_b32 m0, %0" : "=&s"(keep) : "v"(gsrc), "s"(lds_dst) : "memory");
}
template <int N> __device__ __forceinline__ void waitvm() { asm volatile("s_waitcnt vmcnt(%0)" :: "i"(N) : "memory"); }
template <int N, int SLEEP>
__device__ __forceinline__ void poll_units(__amdgpu_buffer_rsrc_t rs, int (&o)[N], unsigned tag, v2u (&u)[N]) {
  _Pragma("nounroll") for (unsigned spins = 0; spins < SPIN_CAP; ++spins) {
    CB();
    v4u t[N];
#pragma unroll
    for (int i = 0; i < N; ++i) t[i] = __builtin_amdgcn_raw_buffer_load_b128(rs, o[i], 0, 16);
    bool pend = false;
#pragma unroll
    for (int i = 0; i < N; ++i) {
      const bool was = o[i] >= 0;
      const bool bad = was & ((t[i][1] != tag) | (t[i][3] != tag));
      u[i][0] = was ? t[i][0] : u[i][0];
      u[i][1] = was ? t[i][2] : u[i][1];
      o[i] = bad ? o[i] : OOB;
      pend |= bad;
    }
    if (!__any(pend)) break;
    if (SLEEP) __builtin_amdgcn_s_sleep(SLEEP);
  }
}
__device__ __forceinline__ void wait_hints(__amdgpu_buffer_rsrc_t rs, int fo, unsigned tag) {
  _Pragma("nounroll") for (unsigned spins = 0; spins < SPIN_CAP; ++spins) {
    CB();
    const v2u f = __builtin_amdgcn_raw_buffer_load_b64(rs, fo, 0, 16);
    fo = ((f[0] == tag) & (f[1] == tag)) ? OOB : fo;
    if (!__any(fo >= 0)) break;
  }
  CB();
}

#define STAMP(i) do {} while (0)

__global__ void __launch_bounds__(512, 2) gqa_fused(const uint16_t* __restrict__ x, const uint16_t* __restrict__ kc, const uint16_t* __restrict__ vc,
                                                     const uint16_t* __restrict__ wq, const uint16_t* __restrict__ wo, uint16_t* out, unsigned char* ws) {
  extern __shared__ __attribute__((aligned(16))) unsigned char smem[];
  LAS unsigned char* L = (LAS unsigned char*)smem;
  LAS unsigned* cnt = (LAS unsigned*)(L + L_CNT);
  STAMP(0);
  const int tid = threadIdx.x, lane = tid & 63, w = __builtin_amdgcn_readfirstlane(tid >> 6), g = lane >> 4, c = lane & 15;
  const int bid = blockIdx.x, kvh = bid & 7, s = (bid >> 3) & 3, b = bid >> 5, G = b >> 2, cb = b & 3;
  unsigned cpf = 0;
  if (w == 7 && lane < 4 && (bid >> 3) < CPFN) cpf = *(const GAS unsigned*)((__builtin_amdgcn_s_getpc() & ~127ull) + (size_t)((bid >> 3) * 4 + lane) * 128);
  const __amdgpu_buffer_rsrc_t rs = __builtin_amdgcn_make_buffer_rsrc((void*)ws, 0, WS_END, 0x00020000);
  const size_t kvbase = ((size_t)(b * 8 + kvh) * 2048 + s * 512) * 256;
  const int h = kvh * 4 + s;
  constexpr float C2 = 0.088388347648318440f * 1.4426950408889634f;
  const unsigned ep = __builtin_amdgcn_raw_buffer_load_b32(rs, WS_EPOCH + G * 256, 0, 16);
  const int hh = (cb * 4 + s) & 7, kh = cb >> 1;
  v4u xf[2], wr[8];
  const unsigned lo0 = (w & 3) * 4096 + g * 256;
  const unsigned lb = (unsigned)(uintptr_t)(L + L_K) + (w & 3) * 4096;
#define ROUND(base, rr) _Pragma("unroll") for (int i = 0; i < 4; ++i) dma16((base) + (lo0 + (rr) * 16384 + i * 1024 + ((c ^ (4 * i + g)) << 4)), lb + (rr) * 16384 + i * 1024)
  if (w < 4) {
    const int k0 = 256 * kh + 64 * w + 32 * (c >> 3) + 8 * g;
    const uint16_t* p = wq + (size_t)k0 * 4096 + kvh * 512 + hh * 64 + 8 * (c & 7);
#pragma unroll
    for (int i = 0; i < 8; ++i) wr[i] = *(const GAS v4u*)(p + (size_t)i * 4096);
#pragma unroll
    for (int e = 0; e < 2; ++e) xf[e] = *(const GAS v4u*)(x + (size_t)(16 * G + 8 * e + (c & 7)) * 512 + k0);
    if (tid < 64) cnt[tid] = 0u;
  } else {
    ROUND((const unsigned char*)kc + kvbase, 0);
  }
  RAWBAR();

  if (w >= 4) {
    _Pragma("nounroll") for (int r = 1; r < 16; ++r) {
      if (r == 7) lds_wait<1>(cnt + C_QR, 4u);
      if (r >= 8) lds_wait<1>(cnt + C_KC + (r - 8), 2u);
      const unsigned char* base = (r < 8 ? (const unsigned char*)kc : (const unsigned char*)vc) + kvbase;
      ROUND(base, r & 7);
      if (r >= LAG) { waitvm<4 * LAG>(); lds_signal(cnt + C_R + r - LAG); }
      if (r == 7) STAMP(2);
    }
    STAMP(3);
    if (LAG >= 3) { waitvm<8>(); lds_signal(cnt + C_R + 13); }
    waitvm<4>(); lds_signal(cnt + C_R + 14);
    waitvm<0>(); lds_signal(cnt + C_R + 15);
    STAMP(6);
    if (w == 4) {
      const unsigned tag = __builtin_amdgcn_readfirstlane(ep) + 1u;
      lds_wait<2>(cnt + (F2G == 0 ? C_R + 15 : (F2G == 1 ? C_W : C_AI)), F2G == 2 ? 1u : 4u);
      wait_hints(rs, lane < 32 ? WS_F2 + ((G * 4 + (h >> 3)) * 32 + lane) * 128 : OOB, tag);
      const int yo = WS_Y + (b * 32 + h) * 4096 + lane * 16;
      lds_wait<2>(cnt + C_Y, 4u);
      if (D2S) __builtin_amdgcn_s_sleep(D2S);
      STAMP(11);
      v4u t[4];
      _Pragma("nounroll") for (unsigned spins = 0; spins < SPIN_CAP; ++spins) {
        CB();
#pragma unroll
        for (int i = 0; i < 4; ++i) t[i] = __builtin_amdgcn_raw_buffer_load_b128(rs, yo + i * 1024, 0, 16);
        unsigned mx = 0;
#pragma unroll
        for (int i = 0; i < 4; ++i) mx = max(max(mx, max(t[i][0], t[i][1])), max(t[i][2], t[i][3]));
        if (!__any(mx == EMPTY)) break;
      }
      STAMP(12);
      float v[8];
#pragma unroll
      for (int k = 0; k < 4; ++k) {
        v[2 * k] = (bflo(t[0][k]) + bflo(t[1][k])) + (bflo(t[2][k]) + bflo(t[3][k]));
        v[2 * k + 1] = (bfhi(t[0][k]) + bfhi(t[1][k])) + (bfhi(t[2][k]) + bfhi(t[3][k]));
      }
      float z[4];
#pragma unroll
      for (int k = 0; k < 4; ++k) { const auto sw = __builtin_amdgcn_permlane32_swap(__float_as_uint(v[k]), __float_as_uint(v[k + 4]), false, false); z[k] = __uint_as_float(sw[0]) + __uint_as_float(sw[1]); }
      float q2[2];
#pragma unroll
      for (int k = 0; k < 2; ++k) { const auto sw = __builtin_amdgcn_permlane16_swap(__float_as_uint(z[k]), __float_as_uint(z[k + 2]), false, false); q2[k] = __uint_as_float(sw[0]) + __uint_as_float(sw[1]); }
      const float e0 = q2[0] + dppf<0x128>(q2[0]), e1 = q2[1] + dppf<0x128>(q2[1]);
      const float y = (lane & 8) ? e1 : e0;
      *(GAS uint16_t*)(out + (size_t)(b * 4 + ((lane >> 3) & 3)) * 512 + 16 * h + 2 * (lane & 7) + (lane >> 5)) = (uint16_t)cvtpk(y, y);
      __builtin_amdgcn_raw_buffer_store_b32(tag, rs, (cb == 0 && h == 0 && lane == 0) ? WS_EPOCH + G * 256 : OOB, 0, 16);
      STAMP(13);
    }
  } else {
    {
      v4u fr[8];
      transpose8(wr, fr);
      LAS unsigned char* p = L + L_QP + ((w * 2 + (g >> 1)) * 16 + 4 * (g & 1)) * 256 + (c & 7) * 32;
#pragma unroll
      for (int e = 0; e < 2; ++e) {
        f32x4 acc[8];
#pragma unroll
        for (int j = 0; j < 8; ++j) acc[j] = mfma(xf[e], fr[j], f32x4{0.f, 0.f, 0.f, 0.f});
        if ((g >> 1) == (c >> 3)) {
#pragma unroll
          for (int r = 0; r < 4; ++r) {
            *(LAS f32x4*)(p + (8 * e + r) * 256) = f32x4{acc[0][r], acc[1][r], acc[2][r], acc[3][r]};
            *(LAS f32x4*)(p + (8 * e + r) * 256 + 16) = f32x4{acc[4][r], acc[5][r], acc[6][r], acc[7][r]};
          }
        }
      }
    }
    lds_signal(cnt + C_QP);
    STAMP(1);
    const unsigned tag = __builtin_amdgcn_readfirstlane(ep) + 1u;
    lds_wait<0>(cnt + C_QP, 4u);
    {
      const int r = tid >> 4, nq = tid & 15;
      f32x4 sum = {0.f, 0.f, 0.f, 0.f};
#pragma unroll
      for (int p = 0; p < 8; ++p) sum += *(const LAS f32x4*)(L + L_QP + (p * 16 + r) * 256 + nq * 16);
      lds_signal(cnt + C_QR);
      const v4u unit = {cvtpk(sum[0], sum[1]), tag, cvtpk(sum[2], sum[3]), tag};
      const int row = (hh >> 1) * 4 + (r & 3), dq = (hh & 1) * 16 + nq;
      __builtin_amdgcn_raw_buffer_store_b128(unit, rs, WS_Q + (((4 * G + w) * 8 + kvh) * 2 + kh) * 8192 + (row * 32 + dq) * 16, 0, 16);
    }
    const int opub = WS_O + (G * 32 + kvh * 4 + (c >> 2)) * 16384 + (cb * 4 + s) * 1024 + w * 256 + (c & 3) * 64 + g * 16;
    const int ypub = WS_Y + ((G * 4 + g) * 32 + 8 * cb + 2 * w + (c & 1)) * 4096 + h * 128 + (c >> 1) * 16;
    __builtin_amdgcn_raw_buffer_store_b128(v4u{EMPTY, EMPTY, EMPTY, EMPTY}, rs, opub, 0, 16);
    __builtin_amdgcn_raw_buffer_store_b128(v4u{EMPTY, EMPTY, EMPTY, EMPTY}, rs, ypub, 0, 16);
    STAMP(2);
    v4u kf[32], qt[4];
    const int qo = WS_Q + (b * 8 + kvh) * 16384 + tid * 16;
#pragma unroll
    for (int j = 0; j < 4; ++j) {
      if (j == QJ) {
#pragma unroll
        for (int i = 0; i < 4; ++i) qt[i] = __builtin_amdgcn_raw_buffer_load_b128(rs, qo + i * 4096, 0, 16);
        SB();
      }
      lds_wait<1>(cnt + C_R + 2 * j + (w >> 1), 4u);
#pragma unroll
      for (int t = 0; t < 2; ++t)
#pragma unroll
        for (int stp = 0; stp < 4; ++stp) kf[(2 * j + t) * 4 + stp] = *(const LAS v4u*)(L + L_K + (8 * j + 2 * w + t) * 4096 + c * 256 + (((4 * stp + g) ^ c) << 4));
      lds_signal(cnt + C_KC + 2 * j + (w >> 1));
    }
    STAMP(14);
    {
      v2u u[4]; int o[4];
      bool bad = false;
#pragma unroll
      for (int i = 0; i < 4; ++i) {
        const bool bd = QJ > 3 || (qt[i][1] != tag) | (qt[i][3] != tag);
        u[i] = v2u{qt[i][0], qt[i][2]}; o[i] = bd ? qo + i * 4096 : OOB; bad |= bd;
      }
      if (__any(bad)) poll_units<4, 2>(rs, o, tag, u);
#pragma unroll
      for (int i = 0; i < 2; ++i)
        *(LAS v2u*)(L + L_QI + ((tid >> 5) + 8 * i) * 272 + (tid & 31) * 8) =
            v2u{cvtpk(bflo(u[i][0]) + bflo(u[i + 2][0]), bfhi(u[i][0]) + bfhi(u[i + 2][0])), cvtpk(bflo(u[i][1]) + bflo(u[i + 2][1]), bfhi(u[i][1]) + bfhi(u[i + 2][1]))};
    }
    lds_signal(cnt + C_QI);
    waitvm<0>();
    STAMP(4);
    lds_wait<0>(cnt + C_QI, 4u);
    v4u qf[4];
#pragma unroll
    for (int stp = 0; stp < 4; ++stp) qf[stp] = *(const LAS v4u*)(L + L_QI + c * 272 + stp * 64 + g * 16);
    f32x4 sc[8];
#pragma unroll
    for (int t = 0; t < 8; ++t) sc[t] = f32x4{0.f, 0.f, 0.f, 0.f};
#pragma unroll
    for (int stp = 0; stp < 4; ++stp)
#pragma unroll
      for (int t = 0; t < 8; ++t) sc[t] = mfma(kf[t * 4 + stp], qf[stp], sc[t]);
    float m = sc[0][0];
#pragma unroll
    for (int t = 0; t < 8; ++t)
#pragma unroll
      for (int r = 0; r < 4; ++r) m = fmaxf(m, sc[t][r]);
    m = xor32(xor16(m, true), true);
    if (g == 0) *(LAS float*)(L + L_MAX + (w * 16 + c) * 4) = m;
    lds_signal(cnt + C_MAX);
    lds_wait<0>(cnt + C_MAX, 4u);
#pragma unroll
    for (int ww = 0; ww < 4; ++ww) m = fmaxf(m, *(const LAS float*)(L + L_MAX + (ww * 16 + c) * 4));
    const float ms = m * C2;
    float l0 = 0.f, l1 = 0.f;
#pragma unroll
    for (int t = 0; t < 8; ++t) {
#pragma unroll
      for (int r = 0; r < 4; ++r) sc[t][r] = __builtin_amdgcn_exp2f(fmaf(sc[t][r], C2, -ms));
      l0 += sc[t][0] + sc[t][1]; l1 += sc[t][2] + sc[t][3];
    }
#pragma unroll
    for (int j = 0; j < 4; ++j) {
      const v4u pf = {cvtpk(sc[2 * j][0], sc[2 * j][1]), cvtpk(sc[2 * j][2], sc[2 * j][3]), cvtpk(sc[2 * j + 1][0], sc[2 * j + 1][1]), cvtpk(sc[2 * j + 1][2], sc[2 * j + 1][3])};
      *(LAS v4u*)(L + L_P + (4 * j + w) * 1024 + lane * 16) = pf;
    }
    const float l = xor32(xor16(l0 + l1, false), false);
    if (g == 0) *(LAS float*)(L + L_SUM + (w * 16 + c) * 4) = l;
    lds_signal(cnt + C_P);
    lds_wait<0>(cnt + C_P, 4u);
    STAMP(5);
    v4u pf[16];
#pragma unroll
    for (int i = 0; i < 16; ++i) pf[i] = *(const LAS v4u*)(L + L_P + i * 1024 + lane * 16);
    if (w == 0) {
      float L4 = 0.f;
#pragma unroll
      for (int ww = 0; ww < 4; ++ww) L4 += *(const LAS float*)(L + L_SUM + (ww * 16 + c) * 4);
      __builtin_amdgcn_raw_buffer_store_b64(v2u{tag, tag}, rs, lane == 0 ? WS_F2 + ((G * 4 + cb) * 32 + h) * 128 : OOB, 0, 16);
      const v4u unit = {__float_as_uint(ms), tag, __float_as_uint(L4), tag};
      __builtin_amdgcn_raw_buffer_store_b128(unit, rs, g == 0 ? WS_STAT + (G * 32 + kvh * 4 + (c >> 2)) * 1024 + ((4 * cb + (c & 3)) * 4 + s) * 16 : OOB, 0, 16);
    }
    v4u wf[8], stv;
    const int so = WS_STAT + (G * 32 + h) * 1024 + (w * 16 + c) * 16;
    f32x4 oa[2] = {f32x4{0.f, 0.f, 0.f, 0.f}, f32x4{0.f, 0.f, 0.f, 0.f}};
    {
      const int key = 4 * g + (c >> 2);
      LAS unsigned char* vb0 = L + L_K + key * 256 + (((4 * w + ((c >> 1) & 1)) ^ key) << 4) + (c & 1) * 8;
      LAS unsigned char* vb1 = L + L_K + key * 256 + (((4 * w + 2 + ((c >> 1) & 1)) ^ key) << 4) + (c & 1) * 8;
#pragma unroll
      for (int r = 0; r < 8; ++r) {
        if (r < 6) lds_wait<1>(cnt + C_R + 8 + r, 4u); else lds_wait<0>(cnt + C_R + 8 + r, 4u);
        if (r == 7) STAMP(6);
        v2u a0[4], a1[4];
#pragma unroll
        for (int t = 0; t < 4; ++t) { a0[t] = trread(vb0 + (4 * r + t) * 4096); a1[t] = trread(vb1 + (4 * r + t) * 4096); }
#pragma unroll
        for (int bl = 0; bl < 2; ++bl) {
          oa[0] = mfma(v4u{a0[2 * bl][0], a0[2 * bl][1], a0[2 * bl + 1][0], a0[2 * bl + 1][1]}, pf[2 * r + bl], oa[0]);
          oa[1] = mfma(v4u{a1[2 * bl][0], a1[2 * bl][1], a1[2 * bl + 1][0], a1[2 * bl + 1][1]}, pf[2 * r + bl], oa[1]);
        }
        if (r == 2) lds_signal(cnt + C_PVH);
        if (r == WOR) {
          const uint16_t* wop = wo + (size_t)(h * 128 + 32 * w + 4 * g) * 512 + 128 * cb + 8 * c;
#pragma unroll
          for (int i = 0; i < 8; ++i) wf[i] = *(const GAS v4u*)(wop + (size_t)(16 * (i >> 2) + (i & 3)) * 512);
          if (STP == 0) stv = __builtin_amdgcn_raw_buffer_load_b128(rs, so, 0, 16);
          SB();
        }
        if (STP == 1 && r == 6) { stv = __builtin_amdgcn_raw_buffer_load_b128(rs, so, 0, 16); SB(); }
      }
    }
    __builtin_amdgcn_raw_buffer_store_b128(v4u{min(cvtpk(oa[0][0], oa[0][1]), EMPTY - 1u), min(cvtpk(oa[0][2], oa[0][3]), EMPTY - 1u),
                                               min(cvtpk(oa[1][0], oa[1][1]), EMPTY - 1u), min(cvtpk(oa[1][2], oa[1][3]), EMPTY - 1u)}, rs, opub, 0, 16);
    if (STP == 2) stv = __builtin_amdgcn_raw_buffer_load_b128(rs, so, 0, 16);
    SB();
    STAMP(8);
    v4u bw[2][4];
    {
      v4u wt[8];
      transpose8(wf, wt);
      lds_wait<0>(cnt + C_PVH, 4u);
#pragma unroll
      for (int j = 0; j < 8; ++j) *(LAS v4u*)(L + L_K + (j * 4 + w) * 1056 + lane * 16) = wt[j];
      lds_signal(cnt + C_W);
      lds_wait<0>(cnt + C_W, 4u);
#pragma unroll
      for (int mm = 0; mm < 2; ++mm)
#pragma unroll
        for (int u = 0; u < 4; ++u) bw[mm][u] = *(const LAS v4u*)(L + L_K + ((c & 7) * 4 + u) * 1056 + (g * 16 + 2 * (2 * w + mm) + (c >> 3)) * 16);
    }
    STAMP(3);
    {
      int o1[1] = {((stv[1] != tag) | (stv[3] != tag)) ? so : OOB}; v2u u1[1] = {v2u{stv[0], stv[2]}};
      if (__any(o1[0] >= 0)) poll_units<1, 0>(rs, o1, tag, u1);
      const float mo = __uint_as_float(u1[0][0]), lo = __uint_as_float(u1[0][1]);
      float M = fmaxf(mo, dppf<0xB1>(mo)); M = fmaxf(M, dppf<0x4E>(M));
      const float e = __builtin_amdgcn_exp2f(mo - M);
      float Lt = e * lo; Lt += dppf<0xB1>(Lt); Lt += dppf<0x4E>(Lt);
      const float wv = e * __builtin_amdgcn_rcpf(Lt);
      const float wg[4] = {dppf<0x00>(wv), dppf<0x55>(wv), dppf<0xAA>(wv), dppf<0xFF>(wv)};
      if (D1S) __builtin_amdgcn_s_sleep(D1S);
      STAMP(9);
      const int oo = WS_O + (G * 32 + h) * 16384 + w * 4096 + lane * 16;
      v4u t[4];
      _Pragma("nounroll") for (unsigned spins = 0; spins < SPIN_CAP; ++spins) {
        CB();
#pragma unroll
        for (int i = 0; i < 4; ++i) t[i] = __builtin_amdgcn_raw_buffer_load_b128(rs, oo + i * 1024, 0, 16);
        unsigned mx = 0;
#pragma unroll
        for (int i = 0; i < 4; ++i) mx = max(max(mx, max(t[i][0], t[i][1])), max(t[i][2], t[i][3]));
        if (!__any(mx == EMPTY)) break;
      }
      STAMP(10);
      v4u o4;
#pragma unroll
      for (int k = 0; k < 4; ++k) {
        float a0 = 0.f, a1 = 0.f;
#pragma unroll
        for (int sr = 0; sr < 4; ++sr) { a0 = fmaf(wg[sr], bflo(t[sr][k]), a0); a1 = fmaf(wg[sr], bfhi(t[sr][k]), a1); }
        o4[k] = cvtpk(a0, a1);
      }
      *(LAS v4u*)(L + L_AI + (4 * w + (c >> 2)) * 272 + g * 64 + (c & 3) * 16) = o4;
    }
    lds_signal(cnt + C_AI);
    lds_wait<0>(cnt + C_AI, 4u);
    STAMP(11);
    v4u af[4];
#pragma unroll
    for (int u2 = 0; u2 < 4; ++u2) af[u2] = *(const LAS v4u*)(L + L_AI + c * 272 + u2 * 64 + g * 16);
    {
      f32x4 y0 = {0.f, 0.f, 0.f, 0.f}, y1 = {0.f, 0.f, 0.f, 0.f};
#pragma unroll
      for (int u2 = 0; u2 < 4; ++u2) { y0 = mfma(af[u2], bw[0][u2], y0); y1 = mfma(af[u2], bw[1][u2], y1); }
      const unsigned p00 = min(cvtpk(y0[0], y0[1]), EMPTY - 1u), p01 = min(cvtpk(y0[2], y0[3]), EMPTY - 1u);
      const unsigned p10 = min(cvtpk(y1[0], y1[1]), EMPTY - 1u), p11 = min(cvtpk(y1[2], y1[3]), EMPTY - 1u);
      const bool odd = c & 1;
      const unsigned r0 = __builtin_amdgcn_mov_dpp(odd ? p00 : p10, 0xB1, 0xf, 0xf, true), r1 = __builtin_amdgcn_mov_dpp(odd ? p01 : p11, 0xB1, 0xf, 0xf, true);
      __builtin_amdgcn_raw_buffer_store_b128(odd ? v4u{r0, r1, p10, p11} : v4u{p00, p01, r0, r1}, rs, ypub, 0, 16);
    }
    lds_signal(cnt + C_Y);
    STAMP(12);
  }
  asm volatile("" :: "v"(cpf));
}

extern "C" void kernel_launch(void* const* d_in, const int* in_sizes, int n_in, void* d_out, int out_size, void* d_ws, size_t ws_size,
                              hipStream_t stream) {
  (void)hipFuncSetAttribute((const void*)gqa_fused, hipFuncAttributeMaxDynamicSharedMemorySize, L_END);
  gqa_fused<<<256, 512, L_END, stream>>>((const uint16_t*)d_in[0], (const uint16_t*)d_in[1], (const uint16_t*)d_in[2], (const uint16_t*)d_in[3],
                                         (const uint16_t*)d_in[4], (uint16_t*)d_out, (unsigned char*)d_ws);
}
```

```cpp
#include <hip/hip_runtime.h>
#include <stdint.h>

#define LAS __attribute__((address_space(3)))
#define GAS __attribute__((address_space(1)))
typedef unsigned v4u __attribute__((ext_vector_type(4)));
typedef unsigned v2u __attribute__((ext_vector_type(2)));
typedef float f32x4 __attribute__((ext_vector_type(4)));
typedef short bf16x8 __attribute__((ext_vector_type(8)));
typedef short s16x4 __attribute__((ext_vector_type(4)));

constexpr int WS_EPOCH = 0;
constexpr int WS_Q = 4096;
constexpr int WS_STAT = WS_Q + 64 * 16384;
constexpr int WS_F2 = WS_STAT + 64 * 1024;
constexpr int WS_O = WS_F2 + 256 * 128;
constexpr int WS_Y = WS_O + 64 * 16384;
constexpr int WS_END = WS_Y + 256 * 4096;
constexpr int WS_STAMP = WS_END;
constexpr unsigned EMPTY = 0xffffffffu;
constexpr int OOB = (int)0x80000000;
constexpr int L_K = 0;
constexpr int L_QP = 114688;
constexpr int L_P = 131072;
constexpr int L_QI = L_P + 16384;
constexpr int L_AI = L_QI + 4352;
constexpr int L_MAX = L_AI + 4352;
constexpr int L_SUM = L_MAX + 256;
constexpr int L_CNT = L_SUM + 256;
constexpr int L_END = L_CNT + 256;
enum { C_QP = 0, C_QI, C_MAX, C_P, C_AI, C_QR, C_W, C_Y, C_PVH, C_R = 16  , C_KC = 32   };
constexpr unsigned SPIN_CAP = 1u << 18;

#define SB() __builtin_amdgcn_sched_barrier(0)
#define CB() asm volatile("" ::: "memory")

__device__ __forceinline__ f32x4 mfma(v4u a, v4u b, f32x4 c) {
  return __builtin_amdgcn_mfma_f32_16x16x32_bf16(__builtin_bit_cast(bf16x8, a), __builtin_bit_cast(bf16x8, b), c, 0, 0, 0);
}
__device__ __forceinline__ unsigned cvtpk(float lo, float hi) {
  typedef float f32x2_ __attribute__((ext_vector_type(2)));
  typedef __bf16 bf16x2_ __attribute__((ext_vector_type(2)));
  return __builtin_bit_cast(unsigned, __builtin_convertvector(f32x2_{lo, hi}, bf16x2_));
}
__device__ __forceinline__ unsigned cvtpay(float lo, float hi) { return min(cvtpk(lo, hi), EMPTY - 1u); }
__device__ __forceinline__ void transpose8(const v4u* in, v4u* out) {
#pragma unroll
  for (int w = 0; w < 4; ++w) {
#pragma unroll
    for (int u = 0; u < 4; ++u) {
      out[2 * w][u] = __builtin_amdgcn_perm(in[2 * u + 1][w], in[2 * u][w], 0x05040100u);
      out[2 * w + 1][u] = __builtin_amdgcn_perm(in[2 * u + 1][w], in[2 * u][w], 0x07060302u);
    }
  }
}
__device__ __forceinline__ v2u trread(LAS unsigned char* p) { return __builtin_bit_cast(v2u, __builtin_amdgcn_ds_read_tr16_b64_v4i16((LAS s16x4*)p)); }
__device__ __forceinline__ float xor16(float x, bool mx) {
  auto s = __builtin_amdgcn_permlane16_swap(__float_as_uint(x), __float_as_uint(x), false, false);
  return mx ? fmaxf(__uint_as_float(s[0]), __uint_as_float(s[1])) : __uint_as_float(s[0]) + __uint_as_float(s[1]);
}
__device__ __forceinline__ float xor32(float x, bool mx) {
  auto s = __builtin_amdgcn_permlane32_swap(__float_as_uint(x), __float_as_uint(x), false, false);
  return mx ? fmaxf(__uint_as_float(s[0]), __uint_as_float(s[1])) : __uint_as_float(s[0]) + __uint_as_float(s[1]);
}
template <int CTRL> __device__ __forceinline__ float dppf(float x) {
  return __builtin_bit_cast(float, __builtin_amdgcn_mov_dpp(__builtin_bit_cast(int, x), CTRL, 0xf, 0xf, true));
}
__device__ __forceinline__ float bflo(unsigned u) { return __uint_as_float(u << 16); }
__device__ __forceinline__ float bfhi(unsigned u) { return __uint_as_float(u & 0xffff0000u); }

__device__ __forceinline__ void lds_signal(LAS unsigned* cnt) {
  const unsigned addr = (unsigned)(uintptr_t)cnt, one = 1u;
  asm volatile("s_mov_b64 exec, 1\n\tds_add_u32 %0, %1\n\ts_mov_b64 exec, -1" :: "v"(addr), "v"(one) : "memory");
}
template <int SLEEP>
__device__ __forceinline__ void lds_wait(LAS unsigned* cnt, unsigned target) {
  CB();
  _Pragma("nounroll") for (unsigned spins = 0; spins < (SPIN_CAP << 4); ++spins) {
    const unsigned v = __builtin_amdgcn_readfirstlane(__hip_atomic_load(cnt, __ATOMIC_RELAXED, __HIP_MEMORY_SCOPE_WORKGROUP));
    if (v >= target) break;
    if (SLEEP) __builtin_amdgcn_s_sleep(SLEEP);
  }
  CB();
}
__device__ __forceinline__ void dma16(const void* gsrc, unsigned lds_dst) {
  unsigned keep;
  asm volatile("s_mov_b32 %0, m0\n\ts_mov_b32 m0, %2\n\ts_nop 0\n\tglobal_load_lds_dwordx4 %1, off nt\n\ts_mov_b32 m0, %0" : "=&s"(keep) : "v"(gsrc), "s"(lds_dst) : "memory");
}
template <int N> __device__ __forceinline__ void waitvm() { asm volatile("s_waitcnt vmcnt(%0)" :: "i"(N) : "memory"); }
template <int N, int SLEEP>
__device__ __forceinline__ void poll_units(__amdgpu_buffer_rsrc_t rs, int (&o)[N], unsigned tag, v2u (&u)[N]) {
  _Pragma("nounroll") for (unsigned spins = 0; spins < SPIN_CAP; ++spins) {
    CB();
    v4u t[N];
#pragma unroll
    for (int i = 0; i < N; ++i) t[i] = __builtin_amdgcn_raw_buffer_load_b128(rs, o[i], 0, 16);
    bool pend = false;
#pragma unroll
    for (int i = 0; i < N; ++i) {
      const bool was = o[i] >= 0;
      const bool bad = was & ((t[i][1] != tag) | (t[i][3] != tag));
      u[i][0] = was ? t[i][0] : u[i][0];
      u[i][1] = was ? t[i][2] : u[i][1];
      o[i] = bad ? o[i] : OOB;
      pend |= bad;
    }
    if (!__any(pend)) break;
    if (SLEEP) __builtin_amdgcn_s_sleep(SLEEP);
  }
  CB();
}
__device__ __forceinline__ void poll_filled(__amdgpu_buffer_rsrc_t rs, int off, v4u (&t)[4]) {
  _Pragma("nounroll") for (unsigned spins = 0; spins < SPIN_CAP; ++spins) {
    CB();
#pragma unroll
    for (int i = 0; i < 4; ++i) t[i] = __builtin_amdgcn_raw_buffer_load_b128(rs, off + i * 1024, 0, 16);
    unsigned mx = 0;
#pragma unroll
    for (int i = 0; i < 4; ++i) mx = max(max(mx, max(t[i][0], t[i][1])), max(t[i][2], t[i][3]));
    if (!__any(mx == EMPTY)) break;
  }
}

#define STAMP(i) do {} while (0)
#define BIDMAP(i) (i)

__global__ void __launch_bounds__(512, 2) gqa_fused(const uint16_t* __restrict__ x, const uint16_t* __restrict__ kc, const uint16_t* __restrict__ vc,
                                                     const uint16_t* __restrict__ wq, const uint16_t* __restrict__ wo, uint16_t* out, unsigned char* ws) {
  extern __shared__ __attribute__((aligned(16))) unsigned char smem[];
  LAS unsigned char* L = (LAS unsigned char*)smem;
  LAS unsigned* cnt = (LAS unsigned*)(L + L_CNT);
  STAMP(0);
  const int tid = threadIdx.x, lane = tid & 63, w = __builtin_amdgcn_readfirstlane(tid >> 6), g = lane >> 4, c = lane & 15;
  const int bid = BIDMAP((int)blockIdx.x), kvh = bid & 7, s = (bid >> 3) & 3, b = bid >> 5, G = b >> 2, cb = b & 3;
  const __amdgpu_buffer_rsrc_t rs = __builtin_amdgcn_make_buffer_rsrc((void*)ws, 0, WS_END, 0x00020000);
  const size_t kvbase = ((size_t)(b * 8 + kvh) * 2048 + s * 512) * 256;
  const int h = kvh * 4 + s;
  constexpr float C2 = 0.088388347648318440f * 1.4426950408889634f;
  const unsigned ep = __builtin_amdgcn_raw_buffer_load_b32(rs, WS_EPOCH + G * 256, 0, 16);
  const int hh = (cb * 4 + s) & 7, kh = cb >> 1;
  v4u xf[2], wr[8];
  const unsigned lo0 = (w & 3) * 4096 + g * 256;
  const unsigned lb = (unsigned)(uintptr_t)(L + L_K) + (w & 3) * 4096;
#define ROUND(base, rr) _Pragma("unroll") for (int i = 0; i < 4; ++i) dma16((base) + (lo0 + (rr) * 16384 + i * 1024 + ((c ^ (4 * i + g)) << 4)), lb + (rr) * 16384 + i * 1024)
  if (w < 4) {
    const int k0 = 256 * kh + 64 * w + 32 * (c >> 3) + 8 * g;
    const uint16_t* p = wq + (size_t)k0 * 4096 + kvh * 512 + hh * 64 + 8 * (c & 7);
#pragma unroll
    for (int i = 0; i < 8; ++i) wr[i] = *(const GAS v4u*)(p + (size_t)i * 4096);
#pragma unroll
    for (int e = 0; e < 2; ++e) xf[e] = *(const GAS v4u*)(x + (size_t)(16 * G + 8 * e + (c & 7)) * 512 + k0);
    if (tid < 64) cnt[tid] = 0u;
  } else {
    ROUND((const unsigned char*)kc + kvbase, 0);
  }
  SB(); asm volatile("s_waitcnt lgkmcnt(0)" ::: "memory"); __builtin_amdgcn_s_barrier(); CB(); SB();

  if (w >= 4) {
    _Pragma("nounroll") for (int r = 1; r < 16; ++r) {
      if (r == 7) lds_wait<1>(cnt + C_QR, 4u);
      if (r >= 8) lds_wait<1>(cnt + C_KC + (r - 8), 2u);
      const unsigned char* base = (r < 8 ? (const unsigned char*)kc : (const unsigned char*)vc) + kvbase;
      ROUND(base, r & 7);
      if (r >= 2) { waitvm<8>(); lds_signal(cnt + C_R + r - 2); }
    }
    waitvm<4>(); lds_signal(cnt + C_R + 14);
    waitvm<0>(); lds_signal(cnt + C_R + 15);
    STAMP(6);
    if (w == 4) {
      const unsigned tag = __builtin_amdgcn_readfirstlane(ep) + 1u;
      lds_wait<2>(cnt + C_R + 15, 4u);
      {
        int fo = lane < 32 ? WS_F2 + ((G * 4 + (h >> 3)) * 32 + lane) * 128 : OOB;
        _Pragma("nounroll") for (unsigned spins = 0; spins < SPIN_CAP; ++spins) {
          CB();
          const v2u f = __builtin_amdgcn_raw_buffer_load_b64(rs, fo, 0, 16);
          fo = ((f[0] == tag) & (f[1] == tag)) ? OOB : fo;
          if (!__any(fo >= 0)) break;
        }
      }
      lds_wait<2>(cnt + C_Y, 4u);
      __builtin_amdgcn_s_sleep(10);
      STAMP(11);
      v4u t[4];
      poll_filled(rs, WS_Y + (b * 32 + h) * 4096 + lane * 16, t);
      STAMP(12);
      float v[8];
#pragma unroll
      for (int k = 0; k < 4; ++k) {
        v[2 * k] = (bflo(t[0][k]) + bflo(t[1][k])) + (bflo(t[2][k]) + bflo(t[3][k]));
        v[2 * k + 1] = (bfhi(t[0][k]) + bfhi(t[1][k])) + (bfhi(t[2][k]) + bfhi(t[3][k]));
      }
      float z[4], q2[2];
#pragma unroll
      for (int k = 0; k < 4; ++k) { const auto sw = __builtin_amdgcn_permlane32_swap(__float_as_uint(v[k]), __float_as_uint(v[k + 4]), false, false); z[k] = __uint_as_float(sw[0]) + __uint_as_float(sw[1]); }
#pragma unroll
      for (int k = 0; k < 2; ++k) { const auto sw = __builtin_amdgcn_permlane16_swap(__float_as_uint(z[k]), __float_as_uint(z[k + 2]), false, false); q2[k] = __uint_as_float(sw[0]) + __uint_as_float(sw[1]); }
      const float e0 = q2[0] + dppf<0x128>(q2[0]), e1 = q2[1] + dppf<0x128>(q2[1]);
      const float y = (lane & 8) ? e1 : e0;
      { const __amdgpu_buffer_rsrc_t ro = __builtin_amdgcn_make_buffer_rsrc((void*)out, 0, 32768, 0x00020000);
        __builtin_amdgcn_raw_buffer_store_b16((unsigned short)cvtpk(y, y), ro, ((b * 4 + ((lane >> 3) & 3)) * 512 + 16 * h + 2 * (lane & 7) + (lane >> 5)) * 2, 0, 2); }
      __builtin_amdgcn_raw_buffer_store_b32(tag, rs, (cb == 0 && h == 0 && lane == 0) ? WS_EPOCH + G * 256 : OOB, 0, 16);
      STAMP(13);
    }
  } else {
    {
      v4u fr[8];
      transpose8(wr, fr);
      LAS unsigned char* p = L + L_QP + ((w * 2 + (g >> 1)) * 16 + 4 * (g & 1)) * 256 + (c & 7) * 32;
#pragma unroll
      for (int e = 0; e < 2; ++e) {
        f32x4 acc[8];
#pragma unroll
        for (int j = 0; j < 8; ++j) acc[j] = mfma(xf[e], fr[j], f32x4{0.f, 0.f, 0.f, 0.f});
        if ((g >> 1) == (c >> 3)) {
#pragma unroll
          for (int r = 0; r < 4; ++r) {
            *(LAS f32x4*)(p + (8 * e + r) * 256) = f32x4{acc[0][r], acc[1][r], acc[2][r], acc[3][r]};
            *(LAS f32x4*)(p + (8 * e + r) * 256 + 16) = f32x4{acc[4][r], acc[5][r], acc[6][r], acc[7][r]};
          }
        }
      }
    }
    lds_signal(cnt + C_QP);
    const unsigned tag = __builtin_amdgcn_readfirstlane(ep) + 1u;
    lds_wait<0>(cnt + C_QP, 4u);
    {
      const int r = tid >> 4, nq = tid & 15;
      f32x4 sum = {0.f, 0.f, 0.f, 0.f};
#pragma unroll
      for (int p = 0; p < 8; ++p) sum += *(const LAS f32x4*)(L + L_QP + (p * 16 + r) * 256 + nq * 16);
      lds_signal(cnt + C_QR);
      const v4u unit = {cvtpk(sum[0], sum[1]), tag, cvtpk(sum[2], sum[3]), tag};
      const int row = (hh >> 1) * 4 + (r & 3), dq = (hh & 1) * 16 + nq;
      __builtin_amdgcn_raw_buffer_store_b128(unit, rs, WS_Q + (((4 * G + w) * 8 + kvh) * 2 + kh) * 8192 + (row * 32 + dq) * 16, 0, 16);
    }
    STAMP(2);
    const int opub = WS_O + (G * 32 + kvh * 4 + (c >> 2)) * 16384 + (cb * 4 + s) * 1024 + w * 256 + (c & 3) * 64 + g * 16;
    const int ypub = WS_Y + ((G * 4 + g) * 32 + 8 * cb + 2 * w + (c & 1)) * 4096 + h * 128 + (c >> 1) * 16;
    v4u kf[32], qt[4];
    const int qo = WS_Q + (b * 8 + kvh) * 16384 + tid * 16;
#pragma unroll
    for (int j = 0; j < 4; ++j) {
      if (j == 1) {
        __builtin_amdgcn_raw_buffer_store_b128(v4u{EMPTY, EMPTY, EMPTY, EMPTY}, rs, opub, 0, 16);
        __builtin_amdgcn_raw_buffer_store_b128(v4u{EMPTY, EMPTY, EMPTY, EMPTY}, rs, ypub, 0, 16);
        SB();
      }
      if (j == 3) {
#pragma unroll
        for (int i = 0; i < 4; ++i) qt[i] = __builtin_amdgcn_raw_buffer_load_b128(rs, qo + i * 4096, 0, 16);
        SB();
      }
      lds_wait<1>(cnt + C_R + 2 * j + (w >> 1), 4u);
#pragma unroll
      for (int t = 0; t < 2; ++t)
#pragma unroll
        for (int stp = 0; stp < 4; ++stp) kf[(2 * j + t) * 4 + stp] = *(const LAS v4u*)(L + L_K + (8 * j + 2 * w + t) * 4096 + c * 256 + (((4 * stp + g) ^ c) << 4));
      lds_signal(cnt + C_KC + 2 * j + (w >> 1));
    }
    {
      v2u u[4]; int o[4];
      bool bad = false;
#pragma unroll
      for (int i = 0; i < 4; ++i) {
        const bool bd = (qt[i][1] != tag) | (qt[i][3] != tag);
        u[i] = v2u{qt[i][0], qt[i][2]}; o[i] = bd ? qo + i * 4096 : OOB; bad |= bd;
      }
      if (__any(bad)) poll_units<4, 2>(rs, o, tag, u);
#pragma unroll
      for (int i = 0; i < 2; ++i)
        *(LAS v2u*)(L + L_QI + ((tid >> 5) + 8 * i) * 272 + (tid & 31) * 8) =
            v2u{cvtpk(bflo(u[i][0]) + bflo(u[i + 2][0]), bfhi(u[i][0]) + bfhi(u[i + 2][0])), cvtpk(bflo(u[i][1]) + bflo(u[i + 2][1]), bfhi(u[i][1]) + bfhi(u[i + 2][1]))};
    }
    lds_signal(cnt + C_QI);
    waitvm<0>();
    STAMP(4);
    lds_wait<0>(cnt + C_QI, 4u);
    v4u qf[4];
#pragma unroll
    for (int stp = 0; stp < 4; ++stp) qf[stp] = *(const LAS v4u*)(L + L_QI + c * 272 + stp * 64 + g * 16);
    f32x4 sc[8];
#pragma unroll
    for (int t = 0; t < 8; ++t) sc[t] = f32x4{0.f, 0.f, 0.f, 0.f};
#pragma unroll
    for (int stp = 0; stp < 4; ++stp)
#pragma unroll
      for (int t = 0; t < 8; ++t) sc[t] = mfma(kf[t * 4 + stp], qf[stp], sc[t]);
    float m = sc[0][0];
#pragma unroll
    for (int t = 0; t < 8; ++t)
#pragma unroll
      for (int r = 0; r < 4; ++r) m = fmaxf(m, sc[t][r]);
    m = xor32(xor16(m, true), true);
    if (g == 0) *(LAS float*)(L + L_MAX + (w * 16 + c) * 4) = m;
    lds_signal(cnt + C_MAX);
    lds_wait<0>(cnt + C_MAX, 4u);
#pragma unroll
    for (int ww = 0; ww < 4; ++ww) m = fmaxf(m, *(const LAS float*)(L + L_MAX + (ww * 16 + c) * 4));
    const float ms = m * C2;
    float l0 = 0.f, l1 = 0.f;
#pragma unroll
    for (int t = 0; t < 8; ++t) {
#pragma unroll
      for (int r = 0; r < 4; ++r) sc[t][r] = __builtin_amdgcn_exp2f(fmaf(sc[t][r], C2, -ms));
      l0 += sc[t][0] + sc[t][1]; l1 += sc[t][2] + sc[t][3];
    }
#pragma unroll
    for (int j = 0; j < 4; ++j) {
      const v4u pf = {cvtpk(sc[2 * j][0], sc[2 * j][1]), cvtpk(sc[2 * j][2], sc[2 * j][3]), cvtpk(sc[2 * j + 1][0], sc[2 * j + 1][1]), cvtpk(sc[2 * j + 1][2], sc[2 * j + 1][3])};
      *(LAS v4u*)(L + L_P + (4 * j + w) * 1024 + lane * 16) = pf;
    }
    const float l = xor32(xor16(l0 + l1, false), false);
    if (g == 0) *(LAS float*)(L + L_SUM + (w * 16 + c) * 4) = l;
    lds_signal(cnt + C_P);
    lds_wait<0>(cnt + C_P, 4u);
    STAMP(5);
    v4u pf[16];
#pragma unroll
    for (int i = 0; i < 16; ++i) pf[i] = *(const LAS v4u*)(L + L_P + i * 1024 + lane * 16);
    if (w == 0) {
      float L4 = 0.f;
#pragma unroll
      for (int ww = 0; ww < 4; ++ww) L4 += *(const LAS float*)(L + L_SUM + (ww * 16 + c) * 4);
      __builtin_amdgcn_raw_buffer_store_b64(v2u{tag, tag}, rs, lane == 0 ? WS_F2 + ((G * 4 + cb) * 32 + h) * 128 : OOB, 0, 16);
      const v4u unit = {__float_as_uint(ms), tag, __float_as_uint(L4), tag};
      __builtin_amdgcn_raw_buffer_store_b128(unit, rs, g == 0 ? WS_STAT + (G * 32 + kvh * 4 + (c >> 2)) * 1024 + ((4 * cb + (c & 3)) * 4 + s) * 16 : OOB, 0, 16);
    }
    v4u wf[8], stv;
    const int so = WS_STAT + (G * 32 + h) * 1024 + (w * 16 + c) * 16;
    f32x4 oa[2] = {f32x4{0.f, 0.f, 0.f, 0.f}, f32x4{0.f, 0.f, 0.f, 0.f}};
    {
      const int key = 4 * g + (c >> 2);
      LAS unsigned char* vb0 = L + L_K + key * 256 + (((4 * w + ((c >> 1) & 1)) ^ key) << 4) + (c & 1) * 8;
      LAS unsigned char* vb1 = L + L_K + key * 256 + (((4 * w + 2 + ((c >> 1) & 1)) ^ key) << 4) + (c & 1) * 8;
#pragma unroll
      for (int r = 0; r < 8; ++r) {
        if (r < 6) lds_wait<1>(cnt + C_R + 8 + r, 4u); else lds_wait<0>(cnt + C_R + 8 + r, 4u);
        v2u a0[4], a1[4];
#pragma unroll
        for (int t = 0; t < 4; ++t) { a0[t] = trread(vb0 + (4 * r + t) * 4096); a1[t] = trread(vb1 + (4 * r + t) * 4096); }
#pragma unroll
        for (int bl = 0; bl < 2; ++bl) {
          oa[0] = mfma(v4u{a0[2 * bl][0], a0[2 * bl][1], a0[2 * bl + 1][0], a0[2 * bl + 1][1]}, pf[2 * r + bl], oa[0]);
          oa[1] = mfma(v4u{a1[2 * bl][0], a1[2 * bl][1], a1[2 * bl + 1][0], a1[2 * bl + 1][1]}, pf[2 * r + bl], oa[1]);
        }
        if (r == 2) lds_signal(cnt + C_PVH);
        if (r == 5) {
          const uint16_t* wop = wo + (size_t)(h * 128 + 32 * w + 4 * g) * 512 + 128 * cb + 8 * c;
#pragma unroll
          for (int i = 0; i < 8; ++i) wf[i] = *(const GAS v4u*)(wop + (size_t)(16 * (i >> 2) + (i & 3)) * 512);
          stv = __builtin_amdgcn_raw_buffer_load_b128(rs, so, 0, 16);
          SB();
        }
      }
    }
    __builtin_amdgcn_raw_buffer_store_b128(v4u{cvtpay(oa[0][0], oa[0][1]), cvtpay(oa[0][2], oa[0][3]), cvtpay(oa[1][0], oa[1][1]), cvtpay(oa[1][2], oa[1][3])}, rs, opub, 0, 16);
    SB();
    STAMP(8);
    v4u bw[2][4];
    {
      v4u wt[8];
      transpose8(wf, wt);
      lds_wait<0>(cnt + C_PVH, 4u);
#pragma unroll
      for (int j = 0; j < 8; ++j) *(LAS v4u*)(L + L_K + (j * 4 + w) * 1056 + lane * 16) = wt[j];
      lds_signal(cnt + C_W);
      lds_wait<0>(cnt + C_W, 4u);
#pragma unroll
      for (int mm = 0; mm < 2; ++mm)
#pragma unroll
        for (int u = 0; u < 4; ++u) bw[mm][u] = *(const LAS v4u*)(L + L_K + ((c & 7) * 4 + u) * 1056 + (g * 16 + 2 * (2 * w + mm) + (c >> 3)) * 16);
    }
    {
      int o1[1] = {((stv[1] != tag) | (stv[3] != tag)) ? so : OOB}; v2u u1[1] = {v2u{stv[0], stv[2]}};
      if (__any(o1[0] >= 0)) poll_units<1, 0>(rs, o1, tag, u1);
      const float mo = __uint_as_float(u1[0][0]), lo = __uint_as_float(u1[0][1]);
      float M = fmaxf(mo, dppf<0xB1>(mo)); M = fmaxf(M, dppf<0x4E>(M));
      const float e = __builtin_amdgcn_exp2f(mo - M);
      float Lt = e * lo; Lt += dppf<0xB1>(Lt); Lt += dppf<0x4E>(Lt);
      const float wv = e * __builtin_amdgcn_rcpf(Lt);
      const float wg[4] = {dppf<0x00>(wv), dppf<0x55>(wv), dppf<0xAA>(wv), dppf<0xFF>(wv)};
      STAMP(9);
      v4u t[4];
      poll_filled(rs, WS_O + (G * 32 + h) * 16384 + w * 4096 + lane * 16, t);
      STAMP(10);
      v4u o4;
#pragma unroll
      for (int k = 0; k < 4; ++k) {
        float a0 = 0.f, a1 = 0.f;
#pragma unroll
        for (int sr = 0; sr < 4; ++sr) { a0 = fmaf(wg[sr], bflo(t[sr][k]), a0); a1 = fmaf(wg[sr], bfhi(t[sr][k]), a1); }
        o4[k] = cvtpk(a0, a1);
      }
      *(LAS v4u*)(L + L_AI + (4 * w + (c >> 2)) * 272 + g * 64 + (c & 3) * 16) = o4;
    }
    lds_signal(cnt + C_AI);
    lds_wait<0>(cnt + C_AI, 4u);
    {
      v4u af[4];
#pragma unroll
      for (int u2 = 0; u2 < 4; ++u2) af[u2] = *(const LAS v4u*)(L + L_AI + c * 272 + u2 * 64 + g * 16);
      f32x4 y0 = {0.f, 0.f, 0.f, 0.f}, y1 = {0.f, 0.f, 0.f, 0.f};
#pragma unroll
      for (int u2 = 0; u2 < 4; ++u2) { y0 = mfma(af[u2], bw[0][u2], y0); y1 = mfma(af[u2], bw[1][u2], y1); }
      const unsigned p00 = cvtpay(y0[0], y0[1]), p01 = cvtpay(y0[2], y0[3]), p10 = cvtpay(y1[0], y1[1]), p11 = cvtpay(y1[2], y1[3]);
      const bool odd = c & 1;
      const unsigned r0 = __builtin_amdgcn_mov_dpp(odd ? p00 : p10, 0xB1, 0xf, 0xf, true), r1 = __builtin_amdgcn_mov_dpp(odd ? p01 : p11, 0xB1, 0xf, 0xf, true);
      __builtin_amdgcn_raw_buffer_store_b128(odd ? v4u{r0, r1, p10, p11} : v4u{p00, p01, r0, r1}, rs, ypub, 0, 16);
    }
    lds_signal(cnt + C_Y);
    STAMP(12);
  }
}

extern "C" void kernel_launch(void* const* d_in, const int* in_sizes, int n_in, void* d_out, int out_size, void* d_ws, size_t ws_size,
                              hipStream_t stream) {
  (void)hipFuncSetAttribute((const void*)gqa_fused, hipFuncAttributeMaxDynamicSharedMemorySize, L_END);
  gqa_fused<<<256, 512, L_END, stream>>>((const uint16_t*)d_in[0], (const uint16_t*)d_in[1], (const uint16_t*)d_in[2], (const uint16_t*)d_in[3],
                                         (const uint16_t*)d_in[4], (uint16_t*)d_out, (unsigned char*)d_ws);
}
```

```cpp
#include <hip/hip_runtime.h>
#include <stdint.h>

#define LAS __attribute__((address_space(3)))
#define GAS __attribute__((address_space(1)))
typedef unsigned v4u __attribute__((ext_vector_type(4)));
typedef unsigned v2u __attribute__((ext_vector_type(2)));
typedef float f32x4 __attribute__((ext_vector_type(4)));
typedef short bf16x8 __attribute__((ext_vector_type(8)));
typedef short s16x4 __attribute__((ext_vector_type(4)));

constexpr int WS_EPOCH = 0;
constexpr int WS_Q = 4096;
constexpr int WS_STAT = WS_Q + 64 * 16384;
constexpr int WS_F2 = WS_STAT + 64 * 1024;
constexpr int WS_O = WS_F2 + 256 * 128;
constexpr int WS_Y = WS_O + 64 * 16384;
constexpr int WS_END = WS_Y + 256 * 4096;
constexpr int WS_STAMP = WS_END;
constexpr unsigned EMPTY = 0xffffffffu;
constexpr int OOB = (int)0x80000000;
constexpr int L_K = 0;
constexpr int L_QP = 114688;
constexpr int L_P = 131072;
constexpr int L_QI = L_P + 16384;
constexpr int L_AI = L_QI + 4352;
constexpr int L_MAX = L_AI + 4352;
constexpr int L_SUM = L_MAX + 256;
constexpr int L_CNT = L_SUM + 256;
constexpr int L_END = L_CNT + 256;
enum { C_QP = 0, C_QI, C_MAX, C_P, C_AI, C_W, C_Y, C_PVH, C_TAG, C_R = 16  , C_QR = 32, C_KC = 33   };
constexpr unsigned SPIN_CAP = 1u << 18;

#define SB() __builtin_amdgcn_sched_barrier(0)
#define CB() asm volatile("" ::: "memory")

__device__ __forceinline__ f32x4 mfma(v4u a, v4u b, f32x4 c) {
  return __builtin_amdgcn_mfma_f32_16x16x32_bf16(__builtin_bit_cast(bf16x8, a), __builtin_bit_cast(bf16x8, b), c, 0, 0, 0);
}
__device__ __forceinline__ unsigned cvtpk(float lo, float hi) {
  typedef float f32x2_ __attribute__((ext_vector_type(2)));
  typedef __bf16 bf16x2_ __attribute__((ext_vector_type(2)));
  return __builtin_bit_cast(unsigned, __builtin_convertvector(f32x2_{lo, hi}, bf16x2_));
}
__device__ __forceinline__ unsigned cvtpay(float lo, float hi) { return min(cvtpk(lo, hi), EMPTY - 1u); }
__device__ __forceinline__ void transpose8(const v4u* in, v4u* out) {
#pragma unroll
  for (int w = 0; w < 4; ++w) {
#pragma unroll
    for (int u = 0; u < 4; ++u) {
      out[2 * w][u] = __builtin_amdgcn_perm(in[2 * u + 1][w], in[2 * u][w], 0x05040100u);
      out[2 * w + 1][u] = __builtin_amdgcn_perm(in[2 * u + 1][w], in[2 * u][w], 0x07060302u);
    }
  }
}
__device__ __forceinline__ v2u trread(LAS unsigned char* p) { return __builtin_bit_cast(v2u, __builtin_amdgcn_ds_read_tr16_b64_v4i16((LAS s16x4*)p)); }
__device__ __forceinline__ float xor16(float x, bool mx) {
  auto s = __builtin_amdgcn_permlane16_swap(__float_as_uint(x), __float_as_uint(x), false, false);
  return mx ? fmaxf(__uint_as_float(s[0]), __uint_as_float(s[1])) : __uint_as_float(s[0]) + __uint_as_float(s[1]);
}
__device__ __forceinline__ float xor32(float x, bool mx) {
  auto s = __builtin_amdgcn_permlane32_swap(__float_as_uint(x), __float_as_uint(x), false, false);
  return mx ? fmaxf(__uint_as_float(s[0]), __uint_as_float(s[1])) : __uint_as_float(s[0]) + __uint_as_float(s[1]);
}
template <int CTRL> __device__ __forceinline__ float dppf(float x) {
  return __builtin_bit_cast(float, __builtin_amdgcn_mov_dpp(__builtin_bit_cast(int, x), CTRL, 0xf, 0xf, true));
}
__device__ __forceinline__ float bflo(unsigned u) { return __uint_as_float(u << 16); }
__device__ __forceinline__ float bfhi(unsigned u) { return __uint_as_float(u & 0xffff0000u); }

__device__ __forceinline__ void lds_signal(LAS unsigned* cnt) {
  const unsigned addr = (unsigned)(uintptr_t)cnt, one = 1u;
  asm volatile("s_mov_b64 exec, 1\n\tds_add_u32 %0, %1\n\ts_mov_b64 exec, -1" :: "v"(addr), "v"(one) : "memory");
}
template <int SLEEP>
__device__ __forceinline__ void lds_wait(LAS unsigned* cnt, unsigned target) {
  CB();
  _Pragma("nounroll") for (unsigned spins = 0; spins < (SPIN_CAP << 4); ++spins) {
    const unsigned v = __builtin_amdgcn_readfirstlane(__hip_atomic_load(cnt, __ATOMIC_RELAXED, __HIP_MEMORY_SCOPE_WORKGROUP));
    if (v >= target) break;
    if (SLEEP) __builtin_amdgcn_s_sleep(SLEEP);
  }
  CB();
}
__device__ __forceinline__ void dma_tile(const void* base, unsigned v0, unsigned v1, unsigned v2, unsigned v3, unsigned lds_dst) {
  unsigned keep;
  asm volatile("s_mov_b32 %0, m0\n\ts_mov_b32 m0, %6\n\ts_nop 0\n\t"
               "global_load_lds_dwordx4 %1, %5 nt\n\tglobal_load_lds_dwordx4 %2, %5 offset:1024 nt\n\t"
               "global_load_lds_dwordx4 %3, %5 offset:2048 nt\n\tglobal_load_lds_dwordx4 %4, %5 offset:3072 nt\n\ts_mov_b32 m0, %0"
               : "=&s"(keep) : "v"(v0), "v"(v1), "v"(v2), "v"(v3), "s"(base), "s"(lds_dst) : "memory");
}
template <int N> __device__ __forceinline__ void waitvm() { asm volatile("s_waitcnt vmcnt(%0)" :: "i"(N) : "memory"); }
template <int N, int SLEEP>
__device__ __forceinline__ void poll_units(__amdgpu_buffer_rsrc_t rs, int (&o)[N], unsigned tag, v2u (&u)[N]) {
  _Pragma("nounroll") for (unsigned spins = 0; spins < SPIN_CAP; ++spins) {
    CB();
    v4u t[N];
#pragma unroll
    for (int i = 0; i < N; ++i) t[i] = __builtin_amdgcn_raw_buffer_load_b128(rs, o[i], 0, 16);
    bool pend = false;
#pragma unroll
    for (int i = 0; i < N; ++i) {
      const bool was = o[i] >= 0;
      const bool bad = was & ((t[i][1] != tag) | (t[i][3] != tag));
      u[i][0] = was ? t[i][0] : u[i][0];
      u[i][1] = was ? t[i][2] : u[i][1];
      o[i] = bad ? o[i] : OOB;
      pend |= bad;
    }
    if (!__any(pend)) break;
    if (SLEEP) __builtin_amdgcn_s_sleep(SLEEP);
  }
  CB();
}
__device__ __forceinline__ void poll_filled(__amdgpu_buffer_rsrc_t rs, int off, v4u (&t)[4]) {
  _Pragma("nounroll") for (unsigned spins = 0; spins < SPIN_CAP; ++spins) {
    CB();
#pragma unroll
    for (int i = 0; i < 4; ++i) t[i] = __builtin_amdgcn_raw_buffer_load_b128(rs, off + i * 1024, 0, 16);
    unsigned mx = 0;
#pragma unroll
    for (int i = 0; i < 4; ++i) mx = max(max(mx, max(t[i][0], t[i][1])), max(t[i][2], t[i][3]));
    if (!__any(mx == EMPTY)) break;
  }
}

#define STAMP(i) do {} while (0)
#define BIDMAP(i) (i)

__global__ void __launch_bounds__(512, 2) gqa_fused(const uint16_t* __restrict__ x, const uint16_t* __restrict__ kc, const uint16_t* __restrict__ vc,
                                                     const uint16_t* __restrict__ wq, const uint16_t* __restrict__ wo, uint16_t* out, unsigned char* ws) {
  extern __shared__ __attribute__((aligned(16))) unsigned char smem[];
  LAS unsigned char* L = (LAS unsigned char*)smem;
  LAS unsigned* cnt = (LAS unsigned*)(L + L_CNT);
  STAMP(0);
  const int tid = threadIdx.x, lane = tid & 63, w = __builtin_amdgcn_readfirstlane(tid >> 6), g = lane >> 4, c = lane & 15;
  const int bid = BIDMAP((int)blockIdx.x), kvh = bid & 7, s = (bid >> 3) & 3, b = bid >> 5, G = b >> 2, cb = b & 3;
#define ARGS() asm volatile("" :: "s"(x), "s"(kc), "s"(vc), "s"(wq), "s"(ws))
  const size_t kvbase = ((size_t)(b * 8 + kvh) * 2048 + s * 512) * 256;
  const int h = kvh * 4 + s;
  constexpr float C2 = 0.088388347648318440f * 1.4426950408889634f;
  const int hh = (cb * 4 + s) & 7, kh = cb >> 1;
#define B0() do { SB(); asm volatile("s_waitcnt lgkmcnt(0)" ::: "memory"); __builtin_amdgcn_s_barrier(); CB(); SB(); } while (0)
  if (__builtin_expect(w >= 4, 1)) {
    const unsigned v0 = g * 256 + ((c ^ g) << 4), v1 = v0 ^ 64u, v2 = v0 ^ 128u, v3 = v0 ^ 192u;
    const unsigned lb = (unsigned)(uintptr_t)(L + L_K) + (w - 4) * 4096;
    const size_t off = kvbase + (w - 4) * 4096;
    ARGS();
    _Pragma("nounroll") for (int r = 0; r < 16; ++r) {
      if (r >= 7) lds_wait<1>(cnt + C_KC + (r - 8), r == 7 ? 4u : 2u);
      dma_tile((r < 8 ? (const unsigned char*)kc : (const unsigned char*)vc) + off + (r & 7) * 16384, v0, v1, v2, v3, lb + (r & 7) * 16384);
      if (r == 2) B0();
      if (r >= 2) { waitvm<8>(); lds_signal(cnt + C_R + r - 2); }
    }
    waitvm<4>(); lds_signal(cnt + C_R + 14);
    waitvm<0>(); lds_signal(cnt + C_R + 15);
    STAMP(6);
  }
  CB();
  if (__builtin_expect(w < 4, 1)) {
    v4u xf[2], wr[8];
    unsigned ep;
    unsigned char* wsp = ws;
    {
      const int k0 = 256 * kh + 64 * w + 32 * (c >> 3) + 8 * g;
      ARGS();
      const uint16_t* p = wq + (size_t)k0 * 4096 + kvh * 512 + hh * 64 + 8 * (c & 7);
#pragma unroll
      for (int i = 0; i < 8; ++i) wr[i] = *(const GAS v4u*)(p + (size_t)i * 4096);
#pragma unroll
      for (int e = 0; e < 2; ++e) xf[e] = *(const GAS v4u*)(x + (size_t)(16 * G + 8 * e + (c & 7)) * 512 + k0);
      asm volatile("" : "+s"(wsp));
      if (tid < 64) cnt[tid] = 0u;
    }
    const __amdgpu_buffer_rsrc_t rs = __builtin_amdgcn_make_buffer_rsrc((void*)wsp, 0, WS_END, 0x00020000);
    ep = __builtin_amdgcn_raw_buffer_load_b32(rs, WS_EPOCH + G * 256, 0, 16);
    B0();
    {
      v4u fr[8];
      transpose8(wr, fr);
      LAS unsigned char* p = L + L_QP + ((w * 2 + (g >> 1)) * 16 + 4 * (g & 1)) * 256 + (c & 7) * 32;
#pragma unroll
      for (int e = 0; e < 2; ++e) {
        f32x4 acc[8];
#pragma unroll
        for (int j = 0; j < 8; ++j) acc[j] = mfma(xf[e], fr[j], f32x4{0.f, 0.f, 0.f, 0.f});
        if ((g >> 1) == (c >> 3)) {
#pragma unroll
          for (int r = 0; r < 4; ++r) {
            *(LAS f32x4*)(p + (8 * e + r) * 256) = f32x4{acc[0][r], acc[1][r], acc[2][r], acc[3][r]};
            *(LAS f32x4*)(p + (8 * e + r) * 256 + 16) = f32x4{acc[4][r], acc[5][r], acc[6][r], acc[7][r]};
          }
        }
      }
    }
    lds_signal(cnt + C_QP);
    const unsigned tag = __builtin_amdgcn_readfirstlane(ep) + 1u;
    if (tid == 0) cnt[C_TAG] = tag;
    lds_wait<0>(cnt + C_QP, 4u);
    {
      const int r = tid >> 4, nq = tid & 15;
      f32x4 sum = {0.f, 0.f, 0.f, 0.f};
#pragma unroll
      for (int p = 0; p < 8; ++p) sum += *(const LAS f32x4*)(L + L_QP + (p * 16 + r) * 256 + nq * 16);
      lds_signal(cnt + C_QR);
      const v4u unit = {cvtpk(sum[0], sum[1]), tag, cvtpk(sum[2], sum[3]), tag};
      const int row = (hh >> 1) * 4 + (r & 3), dq = (hh & 1) * 16 + nq;
      __builtin_amdgcn_raw_buffer_store_b128(unit, rs, WS_Q + (((4 * G + w) * 8 + kvh) * 2 + kh) * 8192 + (row * 32 + dq) * 16, 0, 16);
    }
    STAMP(2);
    const int opub = WS_O + (G * 32 + kvh * 4 + (c >> 2)) * 16384 + (cb * 4 + s) * 1024 + w * 256 + (c & 3) * 64 + g * 16;
    const int ypub = WS_Y + ((G * 4 + g) * 32 + 8 * cb + 2 * w + (c & 1)) * 4096 + h * 128 + (c >> 1) * 16;
    v4u kf[32], qt[4];
    const int qo = WS_Q + (b * 8 + kvh) * 16384 + tid * 16;
#pragma unroll
    for (int j = 0; j < 4; ++j) {
      if (j == 2) {
        __builtin_amdgcn_raw_buffer_store_b128(v4u{EMPTY, EMPTY, EMPTY, EMPTY}, rs, opub, 0, 16);
        __builtin_amdgcn_raw_buffer_store_b128(v4u{EMPTY, EMPTY, EMPTY, EMPTY}, rs, ypub, 0, 16);
        SB();
      }
      if (j == 3) {
#pragma unroll
        for (int i = 0; i < 4; ++i) qt[i] = __builtin_amdgcn_raw_buffer_load_b128(rs, qo + i * 4096, 0, 16);
        SB();
      }
      lds_wait<1>(cnt + C_R + 2 * j + (w >> 1), 4u);
#pragma unroll
      for (int t = 0; t < 2; ++t)
#pragma unroll
        for (int stp = 0; stp < 4; ++stp) kf[(2 * j + t) * 4 + stp] = *(const LAS v4u*)(L + L_K + (8 * j + 2 * w + t) * 4096 + c * 256 + (((4 * stp + g) ^ c) << 4));
      lds_signal(cnt + C_KC + 2 * j + (w >> 1));
    }
    {
      v2u u[4]; int o[4];
      bool bad = false;
#pragma unroll
      for (int i = 0; i < 4; ++i) {
        const bool bd = (qt[i][1] != tag) | (qt[i][3] != tag);
        u[i] = v2u{qt[i][0], qt[i][2]}; o[i] = bd ? qo + i * 4096 : OOB; bad |= bd;
      }
      if (__any(bad)) poll_units<4, 2>(rs, o, tag, u);
#pragma unroll
      for (int i = 0; i < 2; ++i)
        *(LAS v2u*)(L + L_QI + ((tid >> 5) + 8 * i) * 272 + (tid & 31) * 8) =
            v2u{cvtpk(bflo(u[i][0]) + bflo(u[i + 2][0]), bfhi(u[i][0]) + bfhi(u[i + 2][0])), cvtpk(bflo(u[i][1]) + bflo(u[i + 2][1]), bfhi(u[i][1]) + bfhi(u[i + 2][1]))};
    }
    lds_signal(cnt + C_QI);
    waitvm<0>();
    STAMP(4);
    lds_wait<0>(cnt + C_QI, 4u);
    v4u qf[4];
#pragma unroll
    for (int stp = 0; stp < 4; ++stp) qf[stp] = *(const LAS v4u*)(L + L_QI + c * 272 + stp * 64 + g * 16);
    f32x4 sc[8];
#pragma unroll
    for (int t = 0; t < 8; ++t) sc[t] = f32x4{0.f, 0.f, 0.f, 0.f};
#pragma unroll
    for (int stp = 0; stp < 4; ++stp)
#pragma unroll
      for (int t = 0; t < 8; ++t) sc[t] = mfma(kf[t * 4 + stp], qf[stp], sc[t]);
    float m = sc[0][0];
#pragma unroll
    for (int t = 0; t < 8; ++t)
#pragma unroll
      for (int r = 0; r < 4; ++r) m = fmaxf(m, sc[t][r]);
    m = xor32(xor16(m, true), true);
    if (g == 0) *(LAS float*)(L + L_MAX + (w * 16 + c) * 4) = m;
    lds_signal(cnt + C_MAX);
    lds_wait<0>(cnt + C_MAX, 4u);
#pragma unroll
    for (int ww = 0; ww < 4; ++ww) m = fmaxf(m, *(const LAS float*)(L + L_MAX + (ww * 16 + c) * 4));
    const float ms = m * C2;
    float l0 = 0.f, l1 = 0.f;
#pragma unroll
    for (int t = 0; t < 8; ++t) {
#pragma unroll
      for (int r = 0; r < 4; ++r) sc[t][r] = __builtin_amdgcn_exp2f(fmaf(sc[t][r], C2, -ms));
      l0 += sc[t][0] + sc[t][1]; l1 += sc[t][2] + sc[t][3];
    }
#pragma unroll
    for (int j = 0; j < 4; ++j) {
      const v4u pf = {cvtpk(sc[2 * j][0], sc[2 * j][1]), cvtpk(sc[2 * j][2], sc[2 * j][3]), cvtpk(sc[2 * j + 1][0], sc[2 * j + 1][1]), cvtpk(sc[2 * j + 1][2], sc[2 * j + 1][3])};
      *(LAS v4u*)(L + L_P + (4 * j + w) * 1024 + lane * 16) = pf;
    }
    const float l = xor32(xor16(l0 + l1, false), false);
    if (g == 0) *(LAS float*)(L + L_SUM + (w * 16 + c) * 4) = l;
    lds_signal(cnt + C_P);
    lds_wait<0>(cnt + C_P, 4u);
    STAMP(5);
    v4u pf[16];
#pragma unroll
    for (int i = 0; i < 16; ++i) pf[i] = *(const LAS v4u*)(L + L_P + i * 1024 + lane * 16);
    if (w == 0) {
      float L4 = 0.f;
#pragma unroll
      for (int ww = 0; ww < 4; ++ww) L4 += *(const LAS float*)(L + L_SUM + (ww * 16 + c) * 4);
      __builtin_amdgcn_raw_buffer_store_b64(v2u{tag, tag}, rs, lane == 0 ? WS_F2 + ((G * 4 + cb) * 32 + h) * 128 : OOB, 0, 16);
      const v4u unit = {__float_as_uint(ms), tag, __float_as_uint(L4), tag};
      __builtin_amdgcn_raw_buffer_store_b128(unit, rs, g == 0 ? WS_STAT + (G * 32 + kvh * 4 + (c >> 2)) * 1024 + ((4 * cb + (c & 3)) * 4 + s) * 16 : OOB, 0, 16);
    }
    v4u wf[8], stv;
    const int so = WS_STAT + (G * 32 + h) * 1024 + (w * 16 + c) * 16;
    f32x4 oa[2] = {f32x4{0.f, 0.f, 0.f, 0.f}, f32x4{0.f, 0.f, 0.f, 0.f}};
    {
      const int key = 4 * g + (c >> 2);
      LAS unsigned char* vb0 = L + L_K + key * 256 + (((4 * w + ((c >> 1) & 1)) ^ key) << 4) + (c & 1) * 8;
      LAS unsigned char* vb1 = L + L_K + key * 256 + (((4 * w + 2 + ((c >> 1) & 1)) ^ key) << 4) + (c & 1) * 8;
#pragma unroll
      for (int r = 0; r < 8; ++r) {
        if (r < 6) lds_wait<1>(cnt + C_R + 8 + r, 4u); else lds_wait<0>(cnt + C_R + 8 + r, 4u);
        v2u a0[4], a1[4];
#pragma unroll
        for (int t = 0; t < 4; ++t) { a0[t] = trread(vb0 + (4 * r + t) * 4096); a1[t] = trread(vb1 + (4 * r + t) * 4096); }
#pragma unroll
        for (int bl = 0; bl < 2; ++bl) {
          oa[0] = mfma(v4u{a0[2 * bl][0], a0[2 * bl][1], a0[2 * bl + 1][0], a0[2 * bl + 1][1]}, pf[2 * r + bl], oa[0]);
          oa[1] = mfma(v4u{a1[2 * bl][0], a1[2 * bl][1], a1[2 * bl + 1][0], a1[2 * bl + 1][1]}, pf[2 * r + bl], oa[1]);
        }
        if (r == 2) lds_signal(cnt + C_PVH);
        if (r == 5) {
          const uint16_t* wop = wo + (size_t)(h * 128 + 32 * w + 4 * g) * 512 + 128 * cb + 8 * c;
#pragma unroll
          for (int i = 0; i < 8; ++i) wf[i] = *(const GAS v4u*)(wop + (size_t)(16 * (i >> 2) + (i & 3)) * 512);
          stv = __builtin_amdgcn_raw_buffer_load_b128(rs, so, 0, 16);
          SB();
        }
      }
    }
    __builtin_amdgcn_raw_buffer_store_b128(v4u{cvtpay(oa[0][0], oa[0][1]), cvtpay(oa[0][2], oa[0][3]), cvtpay(oa[1][0], oa[1][1]), cvtpay(oa[1][2], oa[1][3])}, rs, opub, 0, 16);
    SB();
    STAMP(8);
    v4u bw[2][4];
    {
      v4u wt[8];
      transpose8(wf, wt);
      lds_wait<0>(cnt + C_PVH, 4u);
#pragma unroll
      for (int j = 0; j < 8; ++j) *(LAS v4u*)(L + L_K + (j * 4 + w) * 1056 + lane * 16) = wt[j];
      lds_signal(cnt + C_W);
      lds_wait<0>(cnt + C_W, 4u);
#pragma unroll
      for (int mm = 0; mm < 2; ++mm)
#pragma unroll
        for (int u = 0; u < 4; ++u) bw[mm][u] = *(const LAS v4u*)(L + L_K + ((c & 7) * 4 + u) * 1056 + (g * 16 + 2 * (2 * w + mm) + (c >> 3)) * 16);
    }
    {
      int o1[1] = {((stv[1] != tag) | (stv[3] != tag)) ? so : OOB}; v2u u1[1] = {v2u{stv[0], stv[2]}};
      if (__any(o1[0] >= 0)) poll_units<1, 0>(rs, o1, tag, u1);
      const float mo = __uint_as_float(u1[0][0]), lo = __uint_as_float(u1[0][1]);
      float M = fmaxf(mo, dppf<0xB1>(mo)); M = fmaxf(M, dppf<0x4E>(M));
      const float e = __builtin_amdgcn_exp2f(mo - M);
      float Lt = e * lo; Lt += dppf<0xB1>(Lt); Lt += dppf<0x4E>(Lt);
      const float wv = e * __builtin_amdgcn_rcpf(Lt);
      const float wg[4] = {dppf<0x00>(wv), dppf<0x55>(wv), dppf<0xAA>(wv), dppf<0xFF>(wv)};
      __builtin_amdgcn_s_sleep(3);
      STAMP(9);
      v4u t[4];
      poll_filled(rs, WS_O + (G * 32 + h) * 16384 + w * 4096 + lane * 16, t);
      STAMP(10);
      v4u o4;
#pragma unroll
      for (int k = 0; k < 4; ++k) {
        float a0 = 0.f, a1 = 0.f;
#pragma unroll
        for (int sr = 0; sr < 4; ++sr) { a0 = fmaf(wg[sr], bflo(t[sr][k]), a0); a1 = fmaf(wg[sr], bfhi(t[sr][k]), a1); }
        o4[k] = cvtpk(a0, a1);
      }
      *(LAS v4u*)(L + L_AI + (4 * w + (c >> 2)) * 272 + g * 64 + (c & 3) * 16) = o4;
    }
    lds_signal(cnt + C_AI);
    lds_wait<0>(cnt + C_AI, 4u);
    {
      v4u af[4];
#pragma unroll
      for (int u2 = 0; u2 < 4; ++u2) af[u2] = *(const LAS v4u*)(L + L_AI + c * 272 + u2 * 64 + g * 16);
      f32x4 y0 = {0.f, 0.f, 0.f, 0.f}, y1 = {0.f, 0.f, 0.f, 0.f};
#pragma unroll
      for (int u2 = 0; u2 < 4; ++u2) { y0 = mfma(af[u2], bw[0][u2], y0); y1 = mfma(af[u2], bw[1][u2], y1); }
      const unsigned p00 = cvtpay(y0[0], y0[1]), p01 = cvtpay(y0[2], y0[3]), p10 = cvtpay(y1[0], y1[1]), p11 = cvtpay(y1[2], y1[3]);
      const bool odd = c & 1;
      const unsigned r0 = __builtin_amdgcn_mov_dpp(odd ? p00 : p10, 0xB1, 0xf, 0xf, true), r1 = __builtin_amdgcn_mov_dpp(odd ? p01 : p11, 0xB1, 0xf, 0xf, true);
      __builtin_amdgcn_raw_buffer_store_b128(odd ? v4u{r0, r1, p10, p11} : v4u{p00, p01, r0, r1}, rs, ypub, 0, 16);
    }
    lds_signal(cnt + C_Y);
    STAMP(12);
  }
  CB();
  {
    if (w == 4) {
      unsigned char* wsp = ws;
      asm volatile("" : "+s"(wsp));
      const __amdgpu_buffer_rsrc_t rs = __builtin_amdgcn_make_buffer_rsrc((void*)wsp, 0, WS_END, 0x00020000);
      lds_wait<2>(cnt + C_R + 15, 4u);
      const unsigned tag = __builtin_amdgcn_readfirstlane(cnt[C_TAG]);
      {
        int fo = lane < 32 ? WS_F2 + ((G * 4 + (h >> 3)) * 32 + lane) * 128 : OOB;
        _Pragma("nounroll") for (unsigned spins = 0; spins < SPIN_CAP; ++spins) {
          CB();
          const v2u f = __builtin_amdgcn_raw_buffer_load_b64(rs, fo, 0, 16);
          fo = ((f[0] == tag) & (f[1] == tag)) ? OOB : fo;
          if (!__any(fo >= 0)) break;
        }
      }
      lds_wait<2>(cnt + C_Y, 4u);
      __builtin_amdgcn_s_sleep(6);
      STAMP(11);
      v4u t[4];
      poll_filled(rs, WS_Y + (b * 32 + h) * 4096 + lane * 16, t);
      STAMP(12);
      float v[8];
#pragma unroll
      for (int k = 0; k < 4; ++k) {
        v[2 * k] = (bflo(t[0][k]) + bflo(t[1][k])) + (bflo(t[2][k]) + bflo(t[3][k]));
        v[2 * k + 1] = (bfhi(t[0][k]) + bfhi(t[1][k])) + (bfhi(t[2][k]) + bfhi(t[3][k]));
      }
      float z[4], q2[2];
#pragma unroll
      for (int k = 0; k < 4; ++k) { const auto sw = __builtin_amdgcn_permlane32_swap(__float_as_uint(v[k]), __float_as_uint(v[k + 4]), false, false); z[k] = __uint_as_float(sw[0]) + __uint_as_float(sw[1]); }
#pragma unroll
      for (int k = 0; k < 2; ++k) { const auto sw = __builtin_amdgcn_permlane16_swap(__float_as_uint(z[k]), __float_as_uint(z[k + 2]), false, false); q2[k] = __uint_as_float(sw[0]) + __uint_as_float(sw[1]); }
      const float e0 = q2[0] + dppf<0x128>(q2[0]), e1 = q2[1] + dppf<0x128>(q2[1]);
      const float y = (lane & 8) ? e1 : e0;
      *(GAS uint16_t*)(out + (size_t)(b * 4 + ((lane >> 3) & 3)) * 512 + 16 * h + 2 * (lane & 7) + (lane >> 5)) = (uint16_t)cvtpk(y, y);
      __builtin_amdgcn_raw_buffer_store_b32(tag, rs, (cb == 0 && h == 0 && lane == 0) ? WS_EPOCH + G * 256 : OOB, 0, 16);
      STAMP(13);
    }
  }
}

extern "C" void kernel_launch(void* const* d_in, const int* in_sizes, int n_in, void* d_out, int out_size, void* d_ws, size_t ws_size,
                              hipStream_t stream) {
  (void)hipFuncSetAttribute((const void*)gqa_fused, hipFuncAttributeMaxDynamicSharedMemorySize, L_END);
  gqa_fused<<<256, 512, L_END, stream>>>((const uint16_t*)d_in[0], (const uint16_t*)d_in[1], (const uint16_t*)d_in[2], (const uint16_t*)d_in[3],
                                         (const uint16_t*)d_in[4], (uint16_t*)d_out, (unsigned char*)d_ws);
}
```

```cpp
#include <hip/hip_runtime.h>
#include <stdint.h>

#define LAS __attribute__((address_space(3)))
#define GAS __attribute__((address_space(1)))
typedef unsigned v4u __attribute__((ext_vector_type(4)));
typedef unsigned v2u __attribute__((ext_vector_type(2)));
typedef float f32x4 __attribute__((ext_vector_type(4)));
typedef short bf16x8 __attribute__((ext_vector_type(8)));
typedef short s16x4 __attribute__((ext_vector_type(4)));

constexpr int WS_EPOCH = 0;
constexpr int WS_Q = 4096;
constexpr int WS_STAT = WS_Q + 64 * 16384;
constexpr int WS_F2 = WS_STAT + 64 * 1024;
constexpr int WS_O = WS_F2 + 256 * 128;
constexpr int WS_Y = WS_O + 64 * 16384;
constexpr int WS_END = WS_Y + 256 * 4096;
constexpr int WS_STAMP = WS_END;
constexpr unsigned EMPTY = 0xffffffffu;
constexpr int OOB = (int)0x80000000;
constexpr int L_K = 0;
constexpr int L_QP = 114688;
constexpr int L_P = 131072;
constexpr int L_QI = L_P + 16384;
constexpr int L_AI = L_QI + 4352;
constexpr int L_MAX = L_AI + 4352;
constexpr int L_SUM = L_MAX + 256;
constexpr int L_CNT = L_SUM + 256;
constexpr int L_END = L_CNT + 256;
enum { C_QP = 0, C_QI, C_MAX, C_P, C_AI, C_W, C_Y, C_PVH, C_TAG, C_R = 16  , C_QR = 32, C_KC = 33   };
constexpr unsigned SPIN_CAP = 1u << 18;

#define SB() __builtin_amdgcn_sched_barrier(0)
#define CB() asm volatile("" ::: "memory")

__device__ __forceinline__ f32x4 mfma(v4u a, v4u b, f32x4 c) {
  return __builtin_amdgcn_mfma_f32_16x16x32_bf16(__builtin_bit_cast(bf16x8, a), __builtin_bit_cast(bf16x8, b), c, 0, 0, 0);
}
__device__ __forceinline__ unsigned cvtpk(float lo, float hi) {
  typedef float f32x2_ __attribute__((ext_vector_type(2)));
  typedef __bf16 bf16x2_ __attribute__((ext_vector_type(2)));
  return __builtin_bit_cast(unsigned, __builtin_convertvector(f32x2_{lo, hi}, bf16x2_));
}
__device__ __forceinline__ unsigned cvtpay(float lo, float hi) { return min(cvtpk(lo, hi), EMPTY - 1u); }
__device__ __forceinline__ void transpose8(const v4u* in, v4u* out) {
#pragma unroll
  for (int w = 0; w < 4; ++w) {
#pragma unroll
    for (int u = 0; u < 4; ++u) {
      out[2 * w][u] = __builtin_amdgcn_perm(in[2 * u + 1][w], in[2 * u][w], 0x05040100u);
      out[2 * w + 1][u] = __builtin_amdgcn_perm(in[2 * u + 1][w], in[2 * u][w], 0x07060302u);
    }
  }
}
__device__ __forceinline__ v2u trread(LAS unsigned char* p) { return __builtin_bit_cast(v2u, __builtin_amdgcn_ds_read_tr16_b64_v4i16((LAS s16x4*)p)); }
__device__ __forceinline__ float xor16(float x, bool mx) {
  auto s = __builtin_amdgcn_permlane16_swap(__float_as_uint(x), __float_as_uint(x), false, false);
  return mx ? fmaxf(__uint_as_float(s[0]), __uint_as_float(s[1])) : __uint_as_float(s[0]) + __uint_as_float(s[1]);
}
__device__ __forceinline__ float xor32(float x, bool mx) {
  auto s = __builtin_amdgcn_permlane32_swap(__float_as_uint(x), __float_as_uint(x), false, false);
  return mx ? fmaxf(__uint_as_float(s[0]), __uint_as_float(s[1])) : __uint_as_float(s[0]) + __uint_as_float(s[1]);
}
template <int CTRL> __device__ __forceinline__ float dppf(float x) {
  return __builtin_bit_cast(float, __builtin_amdgcn_mov_dpp(__builtin_bit_cast(int, x), CTRL, 0xf, 0xf, true));
}
__device__ __forceinline__ float bflo(unsigned u) { return __uint_as_float(u << 16); }
__device__ __forceinline__ float bfhi(unsigned u) { return __uint_as_float(u & 0xffff0000u); }

__device__ __forceinline__ void lds_signal(LAS unsigned* cnt) {
  const unsigned addr = (unsigned)(uintptr_t)cnt, one = 1u;
  asm volatile("s_mov_b64 exec, 1\n\tds_add_u32 %0, %1\n\ts_mov_b64 exec, -1" :: "v"(addr), "v"(one) : "memory");
}
template <int SLEEP>
__device__ __forceinline__ void lds_wait(LAS unsigned* cnt, unsigned target) {
  CB();
  _Pragma("nounroll") for (unsigned spins = 0; spins < (SPIN_CAP << 4); ++spins) {
    const unsigned v = __builtin_amdgcn_readfirstlane(__hip_atomic_load(cnt, __ATOMIC_RELAXED, __HIP_MEMORY_SCOPE_WORKGROUP));
    if (v >= target) break;
    if (SLEEP) __builtin_amdgcn_s_sleep(SLEEP);
  }
  CB();
}
__device__ __forceinline__ void dma_tile(const void* base, unsigned v0, unsigned v1, unsigned v2, unsigned v3, unsigned lds_dst) {
  unsigned keep;
  asm volatile("s_mov_b32 %0, m0\n\ts_mov_b32 m0, %6\n\ts_nop 0\n\t"
               "global_load_lds_dwordx4 %1, %5 nt\n\tglobal_load_lds_dwordx4 %2, %5 offset:1024 nt\n\t"
               "global_load_lds_dwordx4 %3, %5 offset:2048 nt\n\tglobal_load_lds_dwordx4 %4, %5 offset:3072 nt\n\ts_mov_b32 m0, %0"
               : "=&s"(keep) : "v"(v0), "v"(v1), "v"(v2), "v"(v3), "s"(base), "s"(lds_dst) : "memory");
}
template <int N> __device__ __forceinline__ void waitvm() { asm volatile("s_waitcnt vmcnt(%0)" :: "i"(N) : "memory"); }
template <int N, int SLEEP>
__device__ __forceinline__ void poll_units(__amdgpu_buffer_rsrc_t rs, int (&o)[N], unsigned tag, v2u (&u)[N]) {
  _Pragma("nounroll") for (unsigned spins = 0; spins < SPIN_CAP; ++spins) {
    int all = o[0];
#pragma unroll
    for (int i = 1; i < N; ++i) all &= o[i];
    if (!__any(all >= 0)) break;
    if (SLEEP) __builtin_amdgcn_s_sleep(SLEEP);
    CB();
    v4u t[N];
#pragma unroll
    for (int i = 0; i < N; ++i) t[i] = __builtin_amdgcn_raw_buffer_load_b128(rs, o[i], 0, 16);
#pragma unroll
    for (int i = 0; i < N; ++i) {
      const bool ok = (o[i] >= 0) & (t[i][1] == tag) & (t[i][3] == tag);
      u[i][0] = ok ? t[i][0] : u[i][0];
      u[i][1] = ok ? t[i][2] : u[i][1];
      o[i] = ok ? OOB : o[i];
    }
  }
  CB();
}
__device__ __forceinline__ void poll_filled(__amdgpu_buffer_rsrc_t rs, int off, v4u (&t)[4]) {
  _Pragma("nounroll") for (unsigned spins = 0; spins < SPIN_CAP; ++spins) {
    CB();
#pragma unroll
    for (int i = 0; i < 4; ++i) t[i] = __builtin_amdgcn_raw_buffer_load_b128(rs, off + i * 1024, 0, 16);
    unsigned mx = 0;
#pragma unroll
    for (int i = 0; i < 4; ++i) mx = max(max(mx, max(t[i][0], t[i][1])), max(t[i][2], t[i][3]));
    if (!__any(mx == EMPTY)) break;
  }
}

#define STAMP(i) do {} while (0)
#define BIDMAP(i) (i)

__global__ void __launch_bounds__(512, 2) gqa_fused(const uint16_t* __restrict__ x, const uint16_t* __restrict__ kc, const uint16_t* __restrict__ vc,
                                                     const uint16_t* __restrict__ wq, const uint16_t* __restrict__ wo, uint16_t* out, unsigned char* ws) {
  extern __shared__ __attribute__((aligned(16))) unsigned char smem[];
  LAS unsigned char* L = (LAS unsigned char*)smem;
  LAS unsigned* cnt = (LAS unsigned*)(L + L_CNT);
  STAMP(0);
  const int tid = threadIdx.x, lane = tid & 63, w = __builtin_amdgcn_readfirstlane(tid >> 6), g = lane >> 4, c = lane & 15;
  const int bid = BIDMAP((int)blockIdx.x), kvh = bid & 7, s = (bid >> 3) & 3, b = bid >> 5, G = b >> 2, cb = b & 3;
#define ARGS() asm volatile("" :: "s"(x), "s"(kc), "s"(vc), "s"(wq), "s"(wo), "s"(out), "s"(ws))
  const size_t kvbase = ((size_t)(b * 8 + kvh) * 2048 + s * 512) * 256;
  const int h = kvh * 4 + s;
  constexpr float C2 = 0.088388347648318440f * 1.4426950408889634f;
  const int hh = (cb * 4 + s) & 7, kh = cb >> 1;
#define B0() do { SB(); asm volatile("s_waitcnt lgkmcnt(0)" ::: "memory"); __builtin_amdgcn_s_barrier(); CB(); SB(); } while (0)
  if (__builtin_expect(w >= 4, 1)) {
    const unsigned v0 = g * 256 + ((c ^ g) << 4), v1 = v0 ^ 64u, v2 = v0 ^ 128u, v3 = v0 ^ 192u;
    const unsigned lb = (unsigned)(uintptr_t)(L + L_K) + (w - 4) * 4096;
    const size_t off = kvbase + (w - 4) * 4096;
    ARGS();
    _Pragma("nounroll") for (int r = 0; r < 16; ++r) {
      if (r >= 7) lds_wait<1>(cnt + C_KC + (r - 8), r == 7 ? 4u : 2u);
      dma_tile((r < 8 ? (const unsigned char*)kc : (const unsigned char*)vc) + off + (r & 7) * 16384, v0, v1, v2, v3, lb + (r & 7) * 16384);
      if (r == 2) B0();
      if (r >= 2) { waitvm<8>(); lds_signal(cnt + C_R + r - 2); }
    }
    waitvm<4>(); lds_signal(cnt + C_R + 14);
    waitvm<0>(); lds_signal(cnt + C_R + 15);
    STAMP(6);
  }
  CB();
  if (__builtin_expect(w < 4, 1)) {
    v4u xf[2], wr[8];
    unsigned ep;
    unsigned char* wsp = ws;
    {
      const int k0 = 256 * kh + 64 * w + 32 * (c >> 3) + 8 * g;
      ARGS();
      const uint16_t* p = wq + (size_t)k0 * 4096 + kvh * 512 + hh * 64 + 8 * (c & 7);
#pragma unroll
      for (int i = 0; i < 8; ++i) wr[i] = *(const GAS v4u*)(p + (size_t)i * 4096);
#pragma unroll
      for (int e = 0; e < 2; ++e) xf[e] = *(const GAS v4u*)(x + (size_t)(16 * G + 8 * e + (c & 7)) * 512 + k0);
      asm volatile("" : "+s"(wsp));
      if (tid < 64) cnt[tid] = 0u;
    }
    const __amdgpu_buffer_rsrc_t rs = __builtin_amdgcn_make_buffer_rsrc((void*)wsp, 0, WS_END, 0x00020000);
    ep = __builtin_amdgcn_raw_buffer_load_b32(rs, WS_EPOCH + G * 256, 0, 16);
    B0();
    {
      v4u fr[8];
      transpose8(wr, fr);
      LAS unsigned char* p = L + L_QP + ((w * 2 + (g >> 1)) * 16 + 4 * (g & 1)) * 256 + (c & 7) * 32;
#pragma unroll
      for (int e = 0; e < 2; ++e) {
        f32x4 acc[8];
#pragma unroll
        for (int j = 0; j < 8; ++j) acc[j] = mfma(xf[e], fr[j], f32x4{0.f, 0.f, 0.f, 0.f});
        if ((g >> 1) == (c >> 3)) {
#pragma unroll
          for (int r = 0; r < 4; ++r) {
            *(LAS f32x4*)(p + (8 * e + r) * 256) = f32x4{acc[0][r], acc[1][r], acc[2][r], acc[3][r]};
            *(LAS f32x4*)(p + (8 * e + r) * 256 + 16) = f32x4{acc[4][r], acc[5][r], acc[6][r], acc[7][r]};
          }
        }
      }
    }
    lds_signal(cnt + C_QP);
    const unsigned tag = __builtin_amdgcn_readfirstlane(ep) + 1u;
    if (tid == 0) cnt[C_TAG] = tag;
    lds_wait<0>(cnt + C_QP, 4u);
    {
      const int r = tid >> 4, nq = tid & 15;
      f32x4 sum = {0.f, 0.f, 0.f, 0.f};
#pragma unroll
      for (int p = 0; p < 8; ++p) sum += *(const LAS f32x4*)(L + L_QP + (p * 16 + r) * 256 + nq * 16);
      lds_signal(cnt + C_QR);
      const v4u unit = {cvtpk(sum[0], sum[1]), tag, cvtpk(sum[2], sum[3]), tag};
      const int row = (hh >> 1) * 4 + (r & 3), dq = (hh & 1) * 16 + nq;
      __builtin_amdgcn_raw_buffer_store_b128(unit, rs, WS_Q + (((4 * G + w) * 8 + kvh) * 2 + kh) * 8192 + (row * 32 + dq) * 16, 0, 16);
    }
    STAMP(2);
    const int opub = WS_O + (G * 32 + kvh * 4 + (c >> 2)) * 16384 + (cb * 4 + s) * 1024 + w * 256 + (c & 3) * 64 + g * 16;
    const int ypub = WS_Y + ((G * 4 + g) * 32 + 8 * cb + 2 * w + (c & 1)) * 4096 + h * 128 + (c >> 1) * 16;
    v4u kf[32], qt[4];
    const int qo = WS_Q + (b * 8 + kvh) * 16384 + tid * 16;
#pragma unroll
    for (int j = 0; j < 4; ++j) {
      if (j == 2) {
        __builtin_amdgcn_raw_buffer_store_b128(v4u{EMPTY, EMPTY, EMPTY, EMPTY}, rs, opub, 0, 16);
        __builtin_amdgcn_raw_buffer_store_b128(v4u{EMPTY, EMPTY, EMPTY, EMPTY}, rs, ypub, 0, 16);
        SB();
      }
      if (j == 3) {
#pragma unroll
        for (int i = 0; i < 4; ++i) qt[i] = __builtin_amdgcn_raw_buffer_load_b128(rs, qo + i * 4096, 0, 16);
        SB();
      }
      lds_wait<1>(cnt + C_R + 2 * j + (w >> 1), 4u);
#pragma unroll
      for (int t = 0; t < 2; ++t)
#pragma unroll
        for (int stp = 0; stp < 4; ++stp) kf[(2 * j + t) * 4 + stp] = *(const LAS v4u*)(L + L_K + (8 * j + 2 * w + t) * 4096 + c * 256 + (((4 * stp + g) ^ c) << 4));
      lds_signal(cnt + C_KC + 2 * j + (w >> 1));
    }
    {
      v2u u[4]; int o[4];
#pragma unroll
      for (int i = 0; i < 4; ++i) { u[i] = v2u{qt[i][0], qt[i][2]}; o[i] = ((qt[i][1] != tag) | (qt[i][3] != tag)) ? qo + i * 4096 : OOB; }
      poll_units<4, 2>(rs, o, tag, u);
#pragma unroll
      for (int i = 0; i < 2; ++i)
        *(LAS v2u*)(L + L_QI + ((tid >> 5) + 8 * i) * 272 + (tid & 31) * 8) =
            v2u{cvtpk(bflo(u[i][0]) + bflo(u[i + 2][0]), bfhi(u[i][0]) + bfhi(u[i + 2][0])), cvtpk(bflo(u[i][1]) + bflo(u[i + 2][1]), bfhi(u[i][1]) + bfhi(u[i + 2][1]))};
    }
    lds_signal(cnt + C_QI);
    waitvm<0>();
    STAMP(4);
    lds_wait<0>(cnt + C_QI, 4u);
    v4u qf[4];
#pragma unroll
    for (int stp = 0; stp < 4; ++stp) qf[stp] = *(const LAS v4u*)(L + L_QI + c * 272 + stp * 64 + g * 16);
    f32x4 sc[8];
#pragma unroll
    for (int t = 0; t < 8; ++t) sc[t] = f32x4{0.f, 0.f, 0.f, 0.f};
#pragma unroll
    for (int stp = 0; stp < 4; ++stp)
#pragma unroll
      for (int t = 0; t < 8; ++t) sc[t] = mfma(kf[t * 4 + stp], qf[stp], sc[t]);
    float m = sc[0][0];
#pragma unroll
    for (int t = 0; t < 8; ++t)
#pragma unroll
      for (int r = 0; r < 4; ++r) m = fmaxf(m, sc[t][r]);
    m = xor32(xor16(m, true), true);
    if (g == 0) *(LAS float*)(L + L_MAX + (w * 16 + c) * 4) = m;
    lds_signal(cnt + C_MAX);
    lds_wait<0>(cnt + C_MAX, 4u);
#pragma unroll
    for (int ww = 0; ww < 4; ++ww) m = fmaxf(m, *(const LAS float*)(L + L_MAX + (ww * 16 + c) * 4));
    const float ms = m * C2;
    float l0 = 0.f, l1 = 0.f;
#pragma unroll
    for (int t = 0; t < 8; ++t) {
#pragma unroll
      for (int r = 0; r < 4; ++r) sc[t][r] = __builtin_amdgcn_exp2f(fmaf(sc[t][r], C2, -ms));
      l0 += sc[t][0] + sc[t][1]; l1 += sc[t][2] + sc[t][3];
    }
#pragma unroll
    for (int j = 0; j < 4; ++j) {
      const v4u pf = {cvtpk(sc[2 * j][0], sc[2 * j][1]), cvtpk(sc[2 * j][2], sc[2 * j][3]), cvtpk(sc[2 * j + 1][0], sc[2 * j + 1][1]), cvtpk(sc[2 * j + 1][2], sc[2 * j + 1][3])};
      *(LAS v4u*)(L + L_P + (4 * j + w) * 1024 + lane * 16) = pf;
    }
    const float l = xor32(xor16(l0 + l1, false), false);
    if (g == 0) *(LAS float*)(L + L_SUM + (w * 16 + c) * 4) = l;
    lds_signal(cnt + C_P);
    lds_wait<0>(cnt + C_P, 4u);
    STAMP(5);
    v4u pf[16];
#pragma unroll
    for (int i = 0; i < 16; ++i) pf[i] = *(const LAS v4u*)(L + L_P + i * 1024 + lane * 16);
    if (w == 0) {
      float L4 = 0.f;
#pragma unroll
      for (int ww = 0; ww < 4; ++ww) L4 += *(const LAS float*)(L + L_SUM + (ww * 16 + c) * 4);
      __builtin_amdgcn_raw_buffer_store_b64(v2u{tag, tag}, rs, lane == 0 ? WS_F2 + ((G * 4 + cb) * 32 + h) * 128 : OOB, 0, 16);
      const v4u unit = {__float_as_uint(ms), tag, __float_as_uint(L4), tag};
      __builtin_amdgcn_raw_buffer_store_b128(unit, rs, g == 0 ? WS_STAT + (G * 32 + kvh * 4 + (c >> 2)) * 1024 + ((4 * cb + (c & 3)) * 4 + s) * 16 : OOB, 0, 16);
    }
    v4u wf[8], stv;
    const int so = WS_STAT + (G * 32 + h) * 1024 + (w * 16 + c) * 16;
    f32x4 oa[2] = {f32x4{0.f, 0.f, 0.f, 0.f}, f32x4{0.f, 0.f, 0.f, 0.f}};
    {
      const int key = 4 * g + (c >> 2);
      LAS unsigned char* vb0 = L + L_K + key * 256 + (((4 * w + ((c >> 1) & 1)) ^ key) << 4) + (c & 1) * 8;
      LAS unsigned char* vb1 = L + L_K + key * 256 + (((4 * w + 2 + ((c >> 1) & 1)) ^ key) << 4) + (c & 1) * 8;
#pragma unroll
      for (int r = 0; r < 8; ++r) {
        if (r < 6) lds_wait<1>(cnt + C_R + 8 + r, 4u); else lds_wait<0>(cnt + C_R + 8 + r, 4u);
        v2u a0[4], a1[4];
#pragma unroll
        for (int t = 0; t < 4; ++t) { a0[t] = trread(vb0 + (4 * r + t) * 4096); a1[t] = trread(vb1 + (4 * r + t) * 4096); }
#pragma unroll
        for (int bl = 0; bl < 2; ++bl) {
          oa[0] = mfma(v4u{a0[2 * bl][0], a0[2 * bl][1], a0[2 * bl + 1][0], a0[2 * bl + 1][1]}, pf[2 * r + bl], oa[0]);
          oa[1] = mfma(v4u{a1[2 * bl][0], a1[2 * bl][1], a1[2 * bl + 1][0], a1[2 * bl + 1][1]}, pf[2 * r + bl], oa[1]);
        }
        if (r == 2) lds_signal(cnt + C_PVH);
        if (r == 5) {
          const uint16_t* wop = wo + (size_t)(h * 128 + 32 * w + 4 * g) * 512 + 128 * cb + 8 * c;
#pragma unroll
          for (int i = 0; i < 8; ++i) wf[i] = *(const GAS v4u*)(wop + (size_t)(16 * (i >> 2) + (i & 3)) * 512);
          stv = __builtin_amdgcn_raw_buffer_load_b128(rs, so, 0, 16);
          SB();
        }
      }
    }
    __builtin_amdgcn_raw_buffer_store_b128(v4u{cvtpay(oa[0][0], oa[0][1]), cvtpay(oa[0][2], oa[0][3]), cvtpay(oa[1][0], oa[1][1]), cvtpay(oa[1][2], oa[1][3])}, rs, opub, 0, 16);
    SB();
    STAMP(8);
    v4u bw[2][4];
    {
      v4u wt[8];
      transpose8(wf, wt);
      lds_wait<0>(cnt + C_PVH, 4u);
#pragma unroll
      for (int j = 0; j < 8; ++j) *(LAS v4u*)(L + L_K + (j * 4 + w) * 1056 + lane * 16) = wt[j];
      lds_signal(cnt + C_W);
      lds_wait<0>(cnt + C_W, 4u);
#pragma unroll
      for (int mm = 0; mm < 2; ++mm)
#pragma unroll
        for (int u = 0; u < 4; ++u) bw[mm][u] = *(const LAS v4u*)(L + L_K + ((c & 7) * 4 + u) * 1056 + (g * 16 + 2 * (2 * w + mm) + (c >> 3)) * 16);
    }
    {
      int o1[1] = {((stv[1] != tag) | (stv[3] != tag)) ? so : OOB}; v2u u1[1] = {v2u{stv[0], stv[2]}};
      poll_units<1, 0>(rs, o1, tag, u1);
      const float mo = __uint_as_float(u1[0][0]), lo = __uint_as_float(u1[0][1]);
      float M = fmaxf(mo, dppf<0xB1>(mo)); M = fmaxf(M, dppf<0x4E>(M));
      const float e = __builtin_amdgcn_exp2f(mo - M);
      float Lt = e * lo; Lt += dppf<0xB1>(Lt); Lt += dppf<0x4E>(Lt);
      const float wv = e * __builtin_amdgcn_rcpf(Lt);
      const float wg[4] = {dppf<0x00>(wv), dppf<0x55>(wv), dppf<0xAA>(wv), dppf<0xFF>(wv)};
      __builtin_amdgcn_s_sleep(3);
      STAMP(9);
      v4u t[4];
      poll_filled(rs, WS_O + (G * 32 + h) * 16384 + w * 4096 + lane * 16, t);
      STAMP(10);
      v4u o4;
#pragma unroll
      for (int k = 0; k < 4; ++k) {
        float a0 = 0.f, a1 = 0.f;
#pragma unroll
        for (int sr = 0; sr < 4; ++sr) { a0 = fmaf(wg[sr], bflo(t[sr][k]), a0); a1 = fmaf(wg[sr], bfhi(t[sr][k]), a1); }
        o4[k] = cvtpk(a0, a1);
      }
      *(LAS v4u*)(L + L_AI + (4 * w + (c >> 2)) * 272 + g * 64 + (c & 3) * 16) = o4;
    }
    lds_signal(cnt + C_AI);
    lds_wait<0>(cnt + C_AI, 4u);
    {
      v4u af[4];
#pragma unroll
      for (int u2 = 0; u2 < 4; ++u2) af[u2] = *(const LAS v4u*)(L + L_AI + c * 272 + u2 * 64 + g * 16);
      f32x4 y0 = {0.f, 0.f, 0.f, 0.f}, y1 = {0.f, 0.f, 0.f, 0.f};
#pragma unroll
      for (int u2 = 0; u2 < 4; ++u2) { y0 = mfma(af[u2], bw[0][u2], y0); y1 = mfma(af[u2], bw[1][u2], y1); }
      const unsigned p00 = cvtpay(y0[0], y0[1]), p01 = cvtpay(y0[2], y0[3]), p10 = cvtpay(y1[0], y1[1]), p11 = cvtpay(y1[2], y1[3]);
      const bool odd = c & 1;
      const unsigned r0 = __builtin_amdgcn_mov_dpp(odd ? p00 : p10, 0xB1, 0xf, 0xf, true), r1 = __builtin_amdgcn_mov_dpp(odd ? p01 : p11, 0xB1, 0xf, 0xf, true);
      __builtin_amdgcn_raw_buffer_store_b128(odd ? v4u{r0, r1, p10, p11} : v4u{p00, p01, r0, r1}, rs, ypub, 0, 16);
    }
    lds_signal(cnt + C_Y);
    STAMP(12);
  }
  CB();
  {
    if (w == 4) {
      unsigned char* wsp = ws;
      asm volatile("" : "+s"(wsp));
      const __amdgpu_buffer_rsrc_t rs = __builtin_amdgcn_make_buffer_rsrc((void*)wsp, 0, WS_END, 0x00020000);
      lds_wait<2>(cnt + C_R + 15, 4u);
      const unsigned tag = __builtin_amdgcn_readfirstlane(cnt[C_TAG]);
      {
        int fo = lane < 32 ? WS_F2 + ((G * 4 + (h >> 3)) * 32 + lane) * 128 : OOB;
        _Pragma("nounroll") for (unsigned spins = 0; spins < SPIN_CAP; ++spins) {
          CB();
          const v2u f = __builtin_amdgcn_raw_buffer_load_b64(rs, fo, 0, 16);
          fo = ((f[0] == tag) & (f[1] == tag)) ? OOB : fo;
          if (!__any(fo >= 0)) break;
        }
      }
      lds_wait<2>(cnt + C_Y, 4u);
      __builtin_amdgcn_s_sleep(6);
      STAMP(11);
      v4u t[4];
      poll_filled(rs, WS_Y + (b * 32 + h) * 4096 + lane * 16, t);
      STAMP(12);
      float v[8];
#pragma unroll
      for (int k = 0; k < 4; ++k) {
        v[2 * k] = (bflo(t[0][k]) + bflo(t[1][k])) + (bflo(t[2][k]) + bflo(t[3][k]));
        v[2 * k + 1] = (bfhi(t[0][k]) + bfhi(t[1][k])) + (bfhi(t[2][k]) + bfhi(t[3][k]));
      }
      float z[4], q2[2];
#pragma unroll
      for (int k = 0; k < 4; ++k) { const auto sw = __builtin_amdgcn_permlane32_swap(__float_as_uint(v[k]), __float_as_uint(v[k + 4]), false, false); z[k] = __uint_as_float(sw[0]) + __uint_as_float(sw[1]); }
#pragma unroll
      for (int k = 0; k < 2; ++k) { const auto sw = __builtin_amdgcn_permlane16_swap(__float_as_uint(z[k]), __float_as_uint(z[k + 2]), false, false); q2[k] = __uint_as_float(sw[0]) + __uint_as_float(sw[1]); }
      const float e0 = q2[0] + dppf<0x128>(q2[0]), e1 = q2[1] + dppf<0x128>(q2[1]);
      const float y = (lane & 8) ? e1 : e0;
      *(GAS uint16_t*)(out + (size_t)(b * 4 + ((lane >> 3) & 3)) * 512 + 16 * h + 2 * (lane & 7) + (lane >> 5)) = (uint16_t)cvtpk(y, y);
      __builtin_amdgcn_raw_buffer_store_b32(tag, rs, (cb == 0 && h == 0 && lane == 0) ? WS_EPOCH + G * 256 : OOB, 0, 16);
      STAMP(13);
    }
  }
}

extern "C" void kernel_launch(void* const* d_in, const int* in_sizes, int n_in, void* d_out, int out_size, void* d_ws, size_t ws_size,
                              hipStream_t stream) {
  (void)hipFuncSetAttribute((const void*)gqa_fused, hipFuncAttributeMaxDynamicSharedMemorySize, L_END);
  gqa_fused<<<256, 512, L_END, stream>>>((const uint16_t*)d_in[0], (const uint16_t*)d_in[1], (const uint16_t*)d_in[2], (const uint16_t*)d_in[3],
                                         (const uint16_t*)d_in[4], (uint16_t*)d_out, (unsigned char*)d_ws);
}
```

```cpp
#define STAMPS 1
#include <hip/hip_runtime.h>
#include <stdint.h>

#define LAS __attribute__((address_space(3)))
#define GAS __attribute__((address_space(1)))
typedef unsigned v4u __attribute__((ext_vector_type(4)));
typedef unsigned v2u __attribute__((ext_vector_type(2)));
typedef float f32x4 __attribute__((ext_vector_type(4)));
typedef short bf16x8 __attribute__((ext_vector_type(8)));
typedef short s16x4 __attribute__((ext_vector_type(4)));

constexpr int WS_EPOCH = 0;
constexpr int WS_Q = 4096;
constexpr int WS_STAT = WS_Q + 64 * 16384;
constexpr int WS_F2 = WS_STAT + 64 * 1024;
constexpr int WS_O = WS_F2 + 256 * 128;
constexpr int WS_Y = WS_O + 64 * 16384;
constexpr int WS_END = WS_Y + 256 * 4096;
constexpr int WS_STAMP = WS_END;
constexpr unsigned EMPTY = 0xffffffffu;
constexpr int OOB = (int)0x80000000;
constexpr int L_K = 0;
constexpr int L_QP = 114688;
constexpr int L_P = 131072;
constexpr int L_QI = L_P + 16384;
constexpr int L_AI = L_QI + 4352;
constexpr int L_MAX = L_AI + 4352;
constexpr int L_SUM = L_MAX + 256;
constexpr int L_CNT = L_SUM + 256;
constexpr int L_END = L_CNT + 256;
enum { C_QP = 0, C_QI, C_MAX, C_P, C_AI, C_W, C_Y, C_PVH, C_TAG, C_R = 16  , C_QR = 32, C_KC = 33   };
constexpr unsigned SPIN_CAP = 1u << 18;

#define SB() __builtin_amdgcn_sched_barrier(0)
#define CB() asm volatile("" ::: "memory")

__device__ __forceinline__ f32x4 mfma(v4u a, v4u b, f32x4 c) {
  return __builtin_amdgcn_mfma_f32_16x16x32_bf16(__builtin_bit_cast(bf16x8, a), __builtin_bit_cast(bf16x8, b), c, 0, 0, 0);
}
__device__ __forceinline__ unsigned cvtpk(float lo, float hi) {
  typedef float f32x2_ __attribute__((ext_vector_type(2)));
  typedef __bf16 bf16x2_ __attribute__((ext_vector_type(2)));
  return __builtin_bit_cast(unsigned, __builtin_convertvector(f32x2_{lo, hi}, bf16x2_));
}
__device__ __forceinline__ unsigned cvtpay(float lo, float hi) { return min(cvtpk(lo, hi), EMPTY - 1u); }
__device__ __forceinline__ void transpose8(const v4u* in, v4u* out) {
#pragma unroll
  for (int w = 0; w < 4; ++w) {
#pragma unroll
    for (int u = 0; u < 4; ++u) {
      out[2 * w][u] = __builtin_amdgcn_perm(in[2 * u + 1][w], in[2 * u][w], 0x05040100u);
      out[2 * w + 1][u] = __builtin_amdgcn_perm(in[2 * u + 1][w], in[2 * u][w], 0x07060302u);
    }
  }
}
__device__ __forceinline__ v2u trread(LAS unsigned char* p) { return __builtin_bit_cast(v2u, __builtin_amdgcn_ds_read_tr16_b64_v4i16((LAS s16x4*)p)); }
__device__ __forceinline__ float xor16(float x, bool mx) {
  auto s = __builtin_amdgcn_permlane16_swap(__float_as_uint(x), __float_as_uint(x), false, false);
  return mx ? fmaxf(__uint_as_float(s[0]), __uint_as_float(s[1])) : __uint_as_float(s[0]) + __uint_as_float(s[1]);
}
__device__ __forceinline__ float xor32(float x, bool mx) {
  auto s = __builtin_amdgcn_permlane32_swap(__float_as_uint(x), __float_as_uint(x), false, false);
  return mx ? fmaxf(__uint_as_float(s[0]), __uint_as_float(s[1])) : __uint_as_float(s[0]) + __uint_as_float(s[1]);
}
template <int CTRL> __device__ __forceinline__ float dppf(float x) {
  return __builtin_bit_cast(float, __builtin_amdgcn_mov_dpp(__builtin_bit_cast(int, x), CTRL, 0xf, 0xf, true));
}
__device__ __forceinline__ float bflo(unsigned u) { return __uint_as_float(u << 16); }
__device__ __forceinline__ float bfhi(unsigned u) { return __uint_as_float(u & 0xffff0000u); }

__device__ __forceinline__ void lds_signal(LAS unsigned* cnt) {
  const unsigned addr = (unsigned)(uintptr_t)cnt, one = 1u;
  asm volatile("s_mov_b64 exec, 1\n\tds_add_u32 %0, %1\n\ts_mov_b64 exec, -1" :: "v"(addr), "v"(one) : "memory");
}
template <int SLEEP>
__device__ __forceinline__ void lds_wait(LAS unsigned* cnt, unsigned target) {
  CB();
  _Pragma("nounroll") for (unsigned spins = 0; spins < (SPIN_CAP << 4); ++spins) {
    const unsigned v = __builtin_amdgcn_readfirstlane(__hip_atomic_load(cnt, __ATOMIC_RELAXED, __HIP_MEMORY_SCOPE_WORKGROUP));
    if (v >= target) break;
    if (SLEEP) __builtin_amdgcn_s_sleep(SLEEP);
  }
  CB();
}
__device__ __forceinline__ void dma_tile(const void* base, unsigned v0, unsigned v1, unsigned v2, unsigned v3, unsigned lds_dst) {
  unsigned keep;
  asm volatile("s_mov_b32 %0, m0\n\ts_mov_b32 m0, %6\n\ts_nop 0\n\t"
               "global_load_lds_dwordx4 %1, %5 nt\n\tglobal_load_lds_dwordx4 %2, %5 offset:1024 nt\n\t"
               "global_load_lds_dwordx4 %3, %5 offset:2048 nt\n\tglobal_load_lds_dwordx4 %4, %5 offset:3072 nt\n\ts_mov_b32 m0, %0"
               : "=&s"(keep) : "v"(v0), "v"(v1), "v"(v2), "v"(v3), "s"(base), "s"(lds_dst) : "memory");
}
template <int N> __device__ __forceinline__ void waitvm() { asm volatile("s_waitcnt vmcnt(%0)" :: "i"(N) : "memory"); }
template <int N, int SLEEP>
__device__ __forceinline__ void poll_units(__amdgpu_buffer_rsrc_t rs, int (&o)[N], unsigned tag, v2u (&u)[N]) {
  _Pragma("nounroll") for (unsigned spins = 0; spins < SPIN_CAP; ++spins) {
    int all = o[0];
#pragma unroll
    for (int i = 1; i < N; ++i) all &= o[i];
    if (!__any(all >= 0)) break;
    if (SLEEP) __builtin_amdgcn_s_sleep(SLEEP);
    CB();
    v4u t[N];
#pragma unroll
    for (int i = 0; i < N; ++i) t[i] = __builtin_amdgcn_raw_buffer_load_b128(rs, o[i], 0, 16);
#pragma unroll
    for (int i = 0; i < N; ++i) {
      const bool ok = (o[i] >= 0) & (t[i][1] == tag) & (t[i][3] == tag);
      u[i][0] = ok ? t[i][0] : u[i][0];
      u[i][1] = ok ? t[i][2] : u[i][1];
      o[i] = ok ? OOB : o[i];
    }
  }
  CB();
}
__device__ __forceinline__ void poll_filled(__amdgpu_buffer_rsrc_t rs, int off, v4u (&t)[4]) {
  _Pragma("nounroll") for (unsigned spins = 0; spins < SPIN_CAP; ++spins) {
    CB();
#pragma unroll
    for (int i = 0; i < 4; ++i) t[i] = __builtin_amdgcn_raw_buffer_load_b128(rs, off + i * 1024, 0, 16);
    unsigned mx = 0;
#pragma unroll
    for (int i = 0; i < 4; ++i) mx = max(max(mx, max(t[i][0], t[i][1])), max(t[i][2], t[i][3]));
    if (!__any(mx == EMPTY)) break;
  }
}

#define STAMP(i) do { unsigned long long t_; SB(); asm volatile("s_memrealtime %0\n\ts_waitcnt lgkmcnt(0)" : "=s"(t_) :: "memory"); SB(); st[i] = t_; } while (0)
#define BIDMAP(i) (i)

__global__ void __launch_bounds__(512, 2) gqa_fused(const uint16_t* __restrict__ x, const uint16_t* __restrict__ kc, const uint16_t* __restrict__ vc,
                                                     const uint16_t* __restrict__ wq, const uint16_t* __restrict__ wo, uint16_t* out, unsigned char* ws) {
  extern __shared__ __attribute__((aligned(16))) unsigned char smem[];
  LAS unsigned char* L = (LAS unsigned char*)smem;
  LAS unsigned* cnt = (LAS unsigned*)(L + L_CNT);
  unsigned long long st[16];
#pragma unroll
  for (int i = 0; i < 16; ++i) st[i] = 0;
  STAMP(0);
  const int tid = threadIdx.x, lane = tid & 63, w = __builtin_amdgcn_readfirstlane(tid >> 6), g = lane >> 4, c = lane & 15;
  const int bid = BIDMAP((int)blockIdx.x), kvh = bid & 7, s = (bid >> 3) & 3, b = bid >> 5, G = b >> 2, cb = b & 3;
#define ARGS() asm volatile("" :: "s"(x), "s"(kc), "s"(vc), "s"(wq), "s"(wo), "s"(out), "s"(ws))
  const size_t kvbase = ((size_t)(b * 8 + kvh) * 2048 + s * 512) * 256;
  const int h = kvh * 4 + s;
  constexpr float C2 = 0.088388347648318440f * 1.4426950408889634f;
  const int hh = (cb * 4 + s) & 7, kh = cb >> 1;
#define B0() do { SB(); asm volatile("s_waitcnt lgkmcnt(0)" ::: "memory"); __builtin_amdgcn_s_barrier(); CB(); SB(); } while (0)
  if (__builtin_expect(w >= 4, 1)) {
    const unsigned v0 = g * 256 + ((c ^ g) << 4), v1 = v0 ^ 64u, v2 = v0 ^ 128u, v3 = v0 ^ 192u;
    const unsigned lb = (unsigned)(uintptr_t)(L + L_K) + (w - 4) * 4096;
    const size_t off = kvbase + (w - 4) * 4096;
    ARGS();
    _Pragma("nounroll") for (int r = 0; r < 16; ++r) {
      if (r >= 7) lds_wait<1>(cnt + C_KC + (r - 8), r == 7 ? 4u : 2u);
      dma_tile((r < 8 ? (const unsigned char*)kc : (const unsigned char*)vc) + off + (r & 7) * 16384, v0, v1, v2, v3, lb + (r & 7) * 16384);
      if (r == 2) B0();
      if (r >= 2) { waitvm<8>(); lds_signal(cnt + C_R + r - 2); }
    }
    waitvm<4>(); lds_signal(cnt + C_R + 14);
    waitvm<0>(); lds_signal(cnt + C_R + 15);
    STAMP(6);
  }
  CB();
  if (__builtin_expect(w < 4, 1)) {
    v4u xf[2], wr[8];
    unsigned ep;
    unsigned char* wsp = ws;
    {
      const int k0 = 256 * kh + 64 * w + 32 * (c >> 3) + 8 * g;
      ARGS();
      const uint16_t* p = wq + (size_t)k0 * 4096 + kvh * 512 + hh * 64 + 8 * (c & 7);
#pragma unroll
      for (int i = 0; i < 8; ++i) wr[i] = *(const GAS v4u*)(p + (size_t)i * 4096);
#pragma unroll
      for (int e = 0; e < 2; ++e) xf[e] = *(const GAS v4u*)(x + (size_t)(16 * G + 8 * e + (c & 7)) * 512 + k0);
      asm volatile("" : "+s"(wsp));
      if (tid < 64) cnt[tid] = 0u;
    }
    const __amdgpu_buffer_rsrc_t rs = __builtin_amdgcn_make_buffer_rsrc((void*)wsp, 0, WS_END, 0x00020000);
    ep = __builtin_amdgcn_raw_buffer_load_b32(rs, WS_EPOCH + G * 256, 0, 16);
    B0();
    {
      v4u fr[8];
      transpose8(wr, fr);
      LAS unsigned char* p = L + L_QP + ((w * 2 + (g >> 1)) * 16 + 4 * (g & 1)) * 256 + (c & 7) * 32;
#pragma unroll
      for (int e = 0; e < 2; ++e) {
        f32x4 acc[8];
#pragma unroll
        for (int j = 0; j < 8; ++j) acc[j] = mfma(xf[e], fr[j], f32x4{0.f, 0.f, 0.f, 0.f});
        if ((g >> 1) == (c >> 3)) {
#pragma unroll
          for (int r = 0; r < 4; ++r) {
            *(LAS f32x4*)(p + (8 * e + r) * 256) = f32x4{acc[0][r], acc[1][r], acc[2][r], acc[3][r]};
            *(LAS f32x4*)(p + (8 * e + r) * 256 + 16) = f32x4{acc[4][r], acc[5][r], acc[6][r], acc[7][r]};
          }
        }
      }
    }
    lds_signal(cnt + C_QP);
    const unsigned tag = __builtin_amdgcn_readfirstlane(ep) + 1u;
    if (tid == 0) cnt[C_TAG] = tag;
    lds_wait<0>(cnt + C_QP, 4u);
    {
      const int r = tid >> 4, nq = tid & 15;
      f32x4 sum = {0.f, 0.f, 0.f, 0.f};
#pragma unroll
      for (int p = 0; p < 8; ++p) sum += *(const LAS f32x4*)(L + L_QP + (p * 16 + r) * 256 + nq * 16);
      lds_signal(cnt + C_QR);
      const v4u unit = {cvtpk(sum[0], sum[1]), tag, cvtpk(sum[2], sum[3]), tag};
      const int row = (hh >> 1) * 4 + (r & 3), dq = (hh & 1) * 16 + nq;
      __builtin_amdgcn_raw_buffer_store_b128(unit, rs, WS_Q + (((4 * G + w) * 8 + kvh) * 2 + kh) * 8192 + (row * 32 + dq) * 16, 0, 16);
    }
    STAMP(2);
    const int opub = WS_O + (G * 32 + kvh * 4 + (c >> 2)) * 16384 + (cb * 4 + s) * 1024 + w * 256 + (c & 3) * 64 + g * 16;
    const int ypub = WS_Y + ((G * 4 + g) * 32 + 8 * cb + 2 * w + (c & 1)) * 4096 + h * 128 + (c >> 1) * 16;
    v4u kf[32], qt[4];
    const int qo = WS_Q + (b * 8 + kvh) * 16384 + tid * 16;
#pragma unroll
    for (int j = 0; j < 4; ++j) {
      if (j == 2) {
        __builtin_amdgcn_raw_buffer_store_b128(v4u{EMPTY, EMPTY, EMPTY, EMPTY}, rs, opub, 0, 16);
        __builtin_amdgcn_raw_buffer_store_b128(v4u{EMPTY, EMPTY, EMPTY, EMPTY}, rs, ypub, 0, 16);
        SB();
      }
      if (j == 3) {
#pragma unroll
        for (int i = 0; i < 4; ++i) qt[i] = __builtin_amdgcn_raw_buffer_load_b128(rs, qo + i * 4096, 0, 16);
        SB();
      }
      lds_wait<1>(cnt + C_R + 2 * j + (w >> 1), 4u);
#pragma unroll
      for (int t = 0; t < 2; ++t)
#pragma unroll
        for (int stp = 0; stp < 4; ++stp) kf[(2 * j + t) * 4 + stp] = *(const LAS v4u*)(L + L_K + (8 * j + 2 * w + t) * 4096 + c * 256 + (((4 * stp + g) ^ c) << 4));
      lds_signal(cnt + C_KC + 2 * j + (w >> 1));
    }
    {
      v2u u[4]; int o[4];
#pragma unroll
      for (int i = 0; i < 4; ++i) { u[i] = v2u{qt[i][0], qt[i][2]}; o[i] = ((qt[i][1] != tag) | (qt[i][3] != tag)) ? qo + i * 4096 : OOB; }
      poll_units<4, 2>(rs, o, tag, u);
#pragma unroll
      for (int i = 0; i < 2; ++i)
        *(LAS v2u*)(L + L_QI + ((tid >> 5) + 8 * i) * 272 + (tid & 31) * 8) =
            v2u{cvtpk(bflo(u[i][0]) + bflo(u[i + 2][0]), bfhi(u[i][0]) + bfhi(u[i + 2][0])), cvtpk(bflo(u[i][1]) + bflo(u[i + 2][1]), bfhi(u[i][1]) + bfhi(u[i + 2][1]))};
    }
    lds_signal(cnt + C_QI);
    waitvm<0>();
    STAMP(4);
    lds_wait<0>(cnt + C_QI, 4u);
    v4u qf[4];
#pragma unroll
    for (int stp = 0; stp < 4; ++stp) qf[stp] = *(const LAS v4u*)(L + L_QI + c * 272 + stp * 64 + g * 16);
    f32x4 sc[8];
#pragma unroll
    for (int t = 0; t < 8; ++t) sc[t] = f32x4{0.f, 0.f, 0.f, 0.f};
#pragma unroll
    for (int stp = 0; stp < 4; ++stp)
#pragma unroll
      for (int t = 0; t < 8; ++t) sc[t] = mfma(kf[t * 4 + stp], qf[stp], sc[t]);
    float m = sc[0][0];
#pragma unroll
    for (int t = 0; t < 8; ++t)
#pragma unroll
      for (int r = 0; r < 4; ++r) m = fmaxf(m, sc[t][r]);
    m = xor32(xor16(m, true), true);
    if (g == 0) *(LAS float*)(L + L_MAX + (w * 16 + c) * 4) = m;
    lds_signal(cnt + C_MAX);
    lds_wait<0>(cnt + C_MAX, 4u);
#pragma unroll
    for (int ww = 0; ww < 4; ++ww) m = fmaxf(m, *(const LAS float*)(L + L_MAX + (ww * 16 + c) * 4));
    const float ms = m * C2;
    float l0 = 0.f, l1 = 0.f;
#pragma unroll
    for (int t = 0; t < 8; ++t) {
#pragma unroll
      for (int r = 0; r < 4; ++r) sc[t][r] = __builtin_amdgcn_exp2f(fmaf(sc[t][r], C2, -ms));
      l0 += sc[t][0] + sc[t][1]; l1 += sc[t][2] + sc[t][3];
    }
#pragma unroll
    for (int j = 0; j < 4; ++j) {
      const v4u pf = {cvtpk(sc[2 * j][0], sc[2 * j][1]), cvtpk(sc[2 * j][2], sc[2 * j][3]), cvtpk(sc[2 * j + 1][0], sc[2 * j + 1][1]), cvtpk(sc[2 * j + 1][2], sc[2 * j + 1][3])};
      *(LAS v4u*)(L + L_P + (4 * j + w) * 1024 + lane * 16) = pf;
    }
    const float l = xor32(xor16(l0 + l1, false), false);
    if (g == 0) *(LAS float*)(L + L_SUM + (w * 16 + c) * 4) = l;
    lds_signal(cnt + C_P);
    lds_wait<0>(cnt + C_P, 4u);
    STAMP(5);
    v4u pf[16];
#pragma unroll
    for (int i = 0; i < 16; ++i) pf[i] = *(const LAS v4u*)(L + L_P + i * 1024 + lane * 16);
    if (w == 0) {
      float L4 = 0.f;
#pragma unroll
      for (int ww = 0; ww < 4; ++ww) L4 += *(const LAS float*)(L + L_SUM + (ww * 16 + c) * 4);
      __builtin_amdgcn_raw_buffer_store_b64(v2u{tag, tag}, rs, lane == 0 ? WS_F2 + ((G * 4 + cb) * 32 + h) * 128 : OOB, 0, 16);
      const v4u unit = {__float_as_uint(ms), tag, __float_as_uint(L4), tag};
      __builtin_amdgcn_raw_buffer_store_b128(unit, rs, g == 0 ? WS_STAT + (G * 32 + kvh * 4 + (c >> 2)) * 1024 + ((4 * cb + (c & 3)) * 4 + s) * 16 : OOB, 0, 16);
    }
    v4u wf[8], stv;
    const int so = WS_STAT + (G * 32 + h) * 1024 + (w * 16 + c) * 16;
    f32x4 oa[2] = {f32x4{0.f, 0.f, 0.f, 0.f}, f32x4{0.f, 0.f, 0.f, 0.f}};
    {
      const int key = 4 * g + (c >> 2);
      LAS unsigned char* vb0 = L + L_K + key * 256 + (((4 * w + ((c >> 1) & 1)) ^ key) << 4) + (c & 1) * 8;
      LAS unsigned char* vb1 = L + L_K + key * 256 + (((4 * w + 2 + ((c >> 1) & 1)) ^ key) << 4) + (c & 1) * 8;
#pragma unroll
      for (int r = 0; r < 8; ++r) {
        if (r < 6) lds_wait<1>(cnt + C_R + 8 + r, 4u); else lds_wait<0>(cnt + C_R + 8 + r, 4u);
        v2u a0[4], a1[4];
#pragma unroll
        for (int t = 0; t < 4; ++t) { a0[t] = trread(vb0 + (4 * r + t) * 4096); a1[t] = trread(vb1 + (4 * r + t) * 4096); }
#pragma unroll
        for (int bl = 0; bl < 2; ++bl) {
          oa[0] = mfma(v4u{a0[2 * bl][0], a0[2 * bl][1], a0[2 * bl + 1][0], a0[2 * bl + 1][1]}, pf[2 * r + bl], oa[0]);
          oa[1] = mfma(v4u{a1[2 * bl][0], a1[2 * bl][1], a1[2 * bl + 1][0], a1[2 * bl + 1][1]}, pf[2 * r + bl], oa[1]);
        }
        if (r == 2) lds_signal(cnt + C_PVH);
        if (r == 5) {
          const uint16_t* wop = wo + (size_t)(h * 128 + 32 * w + 4 * g) * 512 + 128 * cb + 8 * c;
#pragma unroll
          for (int i = 0; i < 8; ++i) wf[i] = *(const GAS v4u*)(wop + (size_t)(16 * (i >> 2) + (i & 3)) * 512);
          stv = __builtin_amdgcn_raw_buffer_load_b128(rs, so, 0, 16);
          SB();
        }
      }
    }
    __builtin_amdgcn_raw_buffer_store_b128(v4u{cvtpay(oa[0][0], oa[0][1]), cvtpay(oa[0][2], oa[0][3]), cvtpay(oa[1][0], oa[1][1]), cvtpay(oa[1][2], oa[1][3])}, rs, opub, 0, 16);
    SB();
    STAMP(8);
    v4u bw[2][4];
    {
      v4u wt[8];
      transpose8(wf, wt);
      lds_wait<0>(cnt + C_PVH, 4u);
#pragma unroll
      for (int j = 0; j < 8; ++j) *(LAS v4u*)(L + L_K + (j * 4 + w) * 1056 + lane * 16) = wt[j];
      lds_signal(cnt + C_W);
      lds_wait<0>(cnt + C_W, 4u);
#pragma unroll
      for (int mm = 0; mm < 2; ++mm)
#pragma unroll
        for (int u = 0; u < 4; ++u) bw[mm][u] = *(const LAS v4u*)(L + L_K + ((c & 7) * 4 + u) * 1056 + (g * 16 + 2 * (2 * w + mm) + (c >> 3)) * 16);
    }
    {
      int o1[1] = {((stv[1] != tag) | (stv[3] != tag)) ? so : OOB}; v2u u1[1] = {v2u{stv[0], stv[2]}};
      poll_units<1, 0>(rs, o1, tag, u1);
      const float mo = __uint_as_float(u1[0][0]), lo = __uint_as_float(u1[0][1]);
      float M = fmaxf(mo, dppf<0xB1>(mo)); M = fmaxf(M, dppf<0x4E>(M));
      const float e = __builtin_amdgcn_exp2f(mo - M);
      float Lt = e * lo; Lt += dppf<0xB1>(Lt); Lt += dppf<0x4E>(Lt);
      const float wv = e * __builtin_amdgcn_rcpf(Lt);
      const float wg[4] = {dppf<0x00>(wv), dppf<0x55>(wv), dppf<0xAA>(wv), dppf<0xFF>(wv)};
      __builtin_amdgcn_s_sleep(3);
      STAMP(9);
      v4u t[4];
      poll_filled(rs, WS_O + (G * 32 + h) * 16384 + w * 4096 + lane * 16, t);
      STAMP(10);
      v4u o4;
#pragma unroll
      for (int k = 0; k < 4; ++k) {
        float a0 = 0.f, a1 = 0.f;
#pragma unroll
        for (int sr = 0; sr < 4; ++sr) { a0 = fmaf(wg[sr], bflo(t[sr][k]), a0); a1 = fmaf(wg[sr], bfhi(t[sr][k]), a1); }
        o4[k] = cvtpk(a0, a1);
      }
      *(LAS v4u*)(L + L_AI + (4 * w + (c >> 2)) * 272 + g * 64 + (c & 3) * 16) = o4;
    }
    lds_signal(cnt + C_AI);
    lds_wait<0>(cnt + C_AI, 4u);
    {
      v4u af[4];
#pragma unroll
      for (int u2 = 0; u2 < 4; ++u2) af[u2] = *(const LAS v4u*)(L + L_AI + c * 272 + u2 * 64 + g * 16);
      f32x4 y0 = {0.f, 0.f, 0.f, 0.f}, y1 = {0.f, 0.f, 0.f, 0.f};
#pragma unroll
      for (int u2 = 0; u2 < 4; ++u2) { y0 = mfma(af[u2], bw[0][u2], y0); y1 = mfma(af[u2], bw[1][u2], y1); }
      const unsigned p00 = cvtpay(y0[0], y0[1]), p01 = cvtpay(y0[2], y0[3]), p10 = cvtpay(y1[0], y1[1]), p11 = cvtpay(y1[2], y1[3]);
      const bool odd = c & 1;
      const unsigned r0 = __builtin_amdgcn_mov_dpp(odd ? p00 : p10, 0xB1, 0xf, 0xf, true), r1 = __builtin_amdgcn_mov_dpp(odd ? p01 : p11, 0xB1, 0xf, 0xf, true);
      __builtin_amdgcn_raw_buffer_store_b128(odd ? v4u{r0, r1, p10, p11} : v4u{p00, p01, r0, r1}, rs, ypub, 0, 16);
    }
    lds_signal(cnt + C_Y);
    STAMP(12);
  }
  CB();
  {
    if (w == 4) {
      unsigned char* wsp = ws;
      asm volatile("" : "+s"(wsp));
      const __amdgpu_buffer_rsrc_t rs = __builtin_amdgcn_make_buffer_rsrc((void*)wsp, 0, WS_END, 0x00020000);
      lds_wait<2>(cnt + C_R + 15, 4u);
      const unsigned tag = __builtin_amdgcn_readfirstlane(cnt[C_TAG]);
      {
        int fo = lane < 32 ? WS_F2 + ((G * 4 + (h >> 3)) * 32 + lane) * 128 : OOB;
        _Pragma("nounroll") for (unsigned spins = 0; spins < SPIN_CAP; ++spins) {
          CB();
          const v2u f = __builtin_amdgcn_raw_buffer_load_b64(rs, fo, 0, 16);
          fo = ((f[0] == tag) & (f[1] == tag)) ? OOB : fo;
          if (!__any(fo >= 0)) break;
        }
      }
      lds_wait<2>(cnt + C_Y, 4u);
      __builtin_amdgcn_s_sleep(6);
      STAMP(11);
      v4u t[4];
      poll_filled(rs, WS_Y + (b * 32 + h) * 4096 + lane * 16, t);
      STAMP(12);
      float v[8];
#pragma unroll
      for (int k = 0; k < 4; ++k) {
        v[2 * k] = (bflo(t[0][k]) + bflo(t[1][k])) + (bflo(t[2][k]) + bflo(t[3][k]));
        v[2 * k + 1] = (bfhi(t[0][k]) + bfhi(t[1][k])) + (bfhi(t[2][k]) + bfhi(t[3][k]));
      }
      float z[4], q2[2];
#pragma unroll
      for (int k = 0; k < 4; ++k) { const auto sw = __builtin_amdgcn_permlane32_swap(__float_as_uint(v[k]), __float_as_uint(v[k + 4]), false, false); z[k] = __uint_as_float(sw[0]) + __uint_as_float(sw[1]); }
#pragma unroll
      for (int k = 0; k < 2; ++k) { const auto sw = __builtin_amdgcn_permlane16_swap(__float_as_uint(z[k]), __float_as_uint(z[k + 2]), false, false); q2[k] = __uint_as_float(sw[0]) + __uint_as_float(sw[1]); }
      const float e0 = q2[0] + dppf<0x128>(q2[0]), e1 = q2[1] + dppf<0x128>(q2[1]);
      const float y = (lane & 8) ? e1 : e0;
      *(GAS uint16_t*)(out + (size_t)(b * 4 + ((lane >> 3) & 3)) * 512 + 16 * h + 2 * (lane & 7) + (lane >> 5)) = (uint16_t)cvtpk(y, y);
      __builtin_amdgcn_raw_buffer_store_b32(tag, rs, (cb == 0 && h == 0 && lane == 0) ? WS_EPOCH + G * 256 : OOB, 0, 16);
      STAMP(13);
    }
  }
  if (lane == 0) {
    unsigned long long* sp = (unsigned long long*)(ws + WS_STAMP) + (size_t)(bid * 8 + w) * 16;
#pragma unroll
    for (int i = 0; i < 16; ++i) sp[i] = st[i];
  }
}

__device__ __forceinline__ unsigned long long rt_() { unsigned long long t; asm volatile("s_memrealtime %0\n\ts_waitcnt lgkmcnt(0)" : "=s"(t) :: "memory"); return t; }
__global__ void __launch_bounds__(1024) k_delay(const unsigned long long* __restrict__ st, int idx, int mult) {
  __shared__ unsigned long long smn[16], smx[16];
  unsigned long long mn = ~0ull, mx = 0;
  for (int i = threadIdx.x; i < 2048; i += 1024) { mn = min(mn, st[i * 16]); mx = max(mx, st[i * 16 + idx]); }
  for (int o = 32; o; o >>= 1) { mn = min(mn, (unsigned long long)__shfl_xor((long long)mn, o)); mx = max(mx, (unsigned long long)__shfl_xor((long long)mx, o)); }
  if ((threadIdx.x & 63) == 0) { smn[threadIdx.x >> 6] = mn; smx[threadIdx.x >> 6] = mx; }
  __syncthreads();
  if (threadIdx.x >= 64) return;
  for (int i = 0; i < 16; ++i) { mn = min(mn, smn[i]); mx = max(mx, smx[i]); }
  unsigned long long span = mx - mn;
  if (span > 10000) span = 10000;
  const unsigned long long t0 = rt_();
  while (rt_() - t0 < span * (unsigned long long)mult) {}
}
extern "C" void kernel_launch(void* const* d_in, const int* in_sizes, int n_in, void* d_out, int out_size, void* d_ws, size_t ws_size,
                              hipStream_t stream) {
  (void)hipFuncSetAttribute((const void*)gqa_fused, hipFuncAttributeMaxDynamicSharedMemorySize, L_END);
  gqa_fused<<<256, 512, L_END, stream>>>((const uint16_t*)d_in[0], (const uint16_t*)d_in[1], (const uint16_t*)d_in[2], (const uint16_t*)d_in[3],
                                         (const uint16_t*)d_in[4], (uint16_t*)d_out, (unsigned char*)d_ws);
  k_delay<<<1, 1024, 0, stream>>>((const unsigned long long*)((char*)d_ws + WS_STAMP), 13, 0);
}
```

```cpp
#include <hip/hip_runtime.h>
#include <stdint.h>

#define LAS __attribute__((address_space(3)))
#define GAS __attribute__((address_space(1)))
typedef unsigned v4u __attribute__((ext_vector_type(4)));
typedef unsigned v2u __attribute__((ext_vector_type(2)));
typedef float f32x4 __attribute__((ext_vector_type(4)));
typedef short bf16x8 __attribute__((ext_vector_type(8)));
typedef short s16x4 __attribute__((ext_vector_type(4)));

constexpr int WS_EPOCH = 0;
constexpr int WS_Q = 4096;
constexpr int WS_STAT = WS_Q + 64 * 16384;
constexpr int WS_F2 = WS_STAT + 64 * 1024;
constexpr int WS_O = WS_F2 + 256 * 128;
constexpr int WS_Y = WS_O + 64 * 16384;
constexpr int WS_END = WS_Y + 256 * 4096;
constexpr unsigned EMPTY = 0xffffffffu;
constexpr int OOB = (int)0x80000000;
constexpr int L_K = 0;
constexpr int L_QP = 114688;
constexpr int L_P = 131072;
constexpr int L_QI = L_P + 16384;
constexpr int L_AI = L_QI + 4352;
constexpr int L_MAX = L_AI + 4352;
constexpr int L_SUM = L_MAX + 256;
constexpr int L_CNT = L_SUM + 256;
constexpr int L_END = L_CNT + 256;
enum { C_QP = 0, C_QI, C_MAX, C_P, C_AI, C_W, C_Y, C_PVH, C_TAG, C_R = 16  , C_QR = 32, C_KC = 33   };
constexpr unsigned SPIN_CAP = 1u << 18;

#define SB() __builtin_amdgcn_sched_barrier(0)
#define CB() asm volatile("" ::: "memory")

__device__ __forceinline__ f32x4 mfma(v4u a, v4u b, f32x4 c) {
  return __builtin_amdgcn_mfma_f32_16x16x32_bf16(__builtin_bit_cast(bf16x8, a), __builtin_bit_cast(bf16x8, b), c, 0, 0, 0);
}
__device__ __forceinline__ unsigned cvtpk(float lo, float hi) {
  typedef float f32x2_ __attribute__((ext_vector_type(2)));
  typedef __bf16 bf16x2_ __attribute__((ext_vector_type(2)));
  return __builtin_bit_cast(unsigned, __builtin_convertvector(f32x2_{lo, hi}, bf16x2_));
}
__device__ __forceinline__ unsigned cvtpay(float lo, float hi) { return min(cvtpk(lo, hi), EMPTY - 1u); }
__device__ __forceinline__ void transpose8(const v4u* in, v4u* out) {
#pragma unroll
  for (int w = 0; w < 4; ++w) {
#pragma unroll
    for (int u = 0; u < 4; ++u) {
      out[2 * w][u] = __builtin_amdgcn_perm(in[2 * u + 1][w], in[2 * u][w], 0x05040100u);
      out[2 * w + 1][u] = __builtin_amdgcn_perm(in[2 * u + 1][w], in[2 * u][w], 0x07060302u);
    }
  }
}
__device__ __forceinline__ v2u trread(LAS unsigned char* p) { return __builtin_bit_cast(v2u, __builtin_amdgcn_ds_read_tr16_b64_v4i16((LAS s16x4*)p)); }
__device__ __forceinline__ float xor16(float x, bool mx) {
  auto s = __builtin_amdgcn_permlane16_swap(__float_as_uint(x), __float_as_uint(x), false, false);
  return mx ? fmaxf(__uint_as_float(s[0]), __uint_as_float(s[1])) : __uint_as_float(s[0]) + __uint_as_float(s[1]);
}
__device__ __forceinline__ float xor32(float x, bool mx) {
  auto s = __builtin_amdgcn_permlane32_swap(__float_as_uint(x), __float_as_uint(x), false, false);
  return mx ? fmaxf(__uint_as_float(s[0]), __uint_as_float(s[1])) : __uint_as_float(s[0]) + __uint_as_float(s[1]);
}
template <int CTRL> __device__ __forceinline__ float dppf(float x) {
  return __builtin_bit_cast(float, __builtin_amdgcn_mov_dpp(__builtin_bit_cast(int, x), CTRL, 0xf, 0xf, true));
}
__device__ __forceinline__ float bflo(unsigned u) { return __uint_as_float(u << 16); }
__device__ __forceinline__ float bfhi(unsigned u) { return __uint_as_float(u & 0xffff0000u); }

__device__ __forceinline__ void lds_signal(LAS unsigned* cnt) {
  const unsigned addr = (unsigned)(uintptr_t)cnt, one = 1u;
  asm volatile("s_mov_b64 exec, 1\n\tds_add_u32 %0, %1\n\ts_mov_b64 exec, -1" :: "v"(addr), "v"(one) : "memory");
}
template <int SLEEP>
__device__ __forceinline__ void lds_wait(LAS unsigned* cnt, unsigned target) {
  CB();
  _Pragma("nounroll") for (unsigned spins = 0; spins < (SPIN_CAP << 4); ++spins) {
    const unsigned v = __builtin_amdgcn_readfirstlane(__hip_atomic_load(cnt, __ATOMIC_RELAXED, __HIP_MEMORY_SCOPE_WORKGROUP));
    if (v >= target) break;
    if (SLEEP) __builtin_amdgcn_s_sleep(SLEEP);
  }
  CB();
}
__device__ __forceinline__ void dma_tile(const void* base, unsigned v0, unsigned v1, unsigned v2, unsigned v3, unsigned lds_dst) {
  unsigned keep;
  asm volatile("s_mov_b32 %0, m0\n\ts_mov_b32 m0, %6\n\ts_nop 0\n\t"
               "global_load_lds_dwordx4 %1, %5 nt\n\tglobal_load_lds_dwordx4 %2, %5 offset:1024 nt\n\t"
               "global_load_lds_dwordx4 %3, %5 offset:2048 nt\n\tglobal_load_lds_dwordx4 %4, %5 offset:3072 nt\n\ts_mov_b32 m0, %0"
               : "=&s"(keep) : "v"(v0), "v"(v1), "v"(v2), "v"(v3), "s"(base), "s"(lds_dst) : "memory");
}
template <int N> __device__ __forceinline__ void waitvm() { asm volatile("s_waitcnt vmcnt(%0)" :: "i"(N) : "memory"); }
template <int N, int SLEEP>
__device__ __forceinline__ void poll_units(__amdgpu_buffer_rsrc_t rs, int (&o)[N], unsigned tag, v2u (&u)[N]) {
  _Pragma("nounroll") for (unsigned spins = 0; spins < SPIN_CAP; ++spins) {
    int all = o[0];
#pragma unroll
    for (int i = 1; i < N; ++i) all &= o[i];
    if (!__any(all >= 0)) break;
    if (SLEEP) __builtin_amdgcn_s_sleep(SLEEP);
    CB();
    v4u t[N];
#pragma unroll
    for (int i = 0; i < N; ++i) t[i] = __builtin_amdgcn_raw_buffer_load_b128(rs, o[i], 0, 16);
#pragma unroll
    for (int i = 0; i < N; ++i) {
      const bool ok = (o[i] >= 0) & (t[i][1] == tag) & (t[i][3] == tag);
      u[i][0] = ok ? t[i][0] : u[i][0];
      u[i][1] = ok ? t[i][2] : u[i][1];
      o[i] = ok ? OOB : o[i];
    }
  }
  CB();
}
__device__ __forceinline__ void poll_filled(__amdgpu_buffer_rsrc_t rs, int off, v4u (&t)[4]) {
  _Pragma("nounroll") for (unsigned spins = 0; spins < SPIN_CAP; ++spins) {
    CB();
#pragma unroll
    for (int i = 0; i < 4; ++i) t[i] = __builtin_amdgcn_raw_buffer_load_b128(rs, off + i * 1024, 0, 16);
    unsigned mx = 0;
#pragma unroll
    for (int i = 0; i < 4; ++i) mx = max(max(mx, max(t[i][0], t[i][1])), max(t[i][2], t[i][3]));
    if (!__any(mx == EMPTY)) break;
  }
}

__global__ void __launch_bounds__(512, 2) gqa_fused(const uint16_t* __restrict__ x, const uint16_t* __restrict__ kc, const uint16_t* __restrict__ vc,
                                                     const uint16_t* __restrict__ wq, const uint16_t* __restrict__ wo, uint16_t* out, unsigned char* ws) {
  extern __shared__ __attribute__((aligned(16))) unsigned char smem[];
  LAS unsigned char* L = (LAS unsigned char*)smem;
  LAS unsigned* cnt = (LAS unsigned*)(L + L_CNT);
  const int tid = threadIdx.x, lane = tid & 63, w = __builtin_amdgcn_readfirstlane(tid >> 6), g = lane >> 4, c = lane & 15;
  const int bid = (int)blockIdx.x, kvh = bid & 7, s = (bid >> 3) & 3, b = bid >> 5, G = b >> 2, cb = b & 3;
#define ARGS() asm volatile("" :: "s"(x), "s"(kc), "s"(vc), "s"(wq), "s"(wo), "s"(out), "s"(ws))
  const size_t kvbase = ((size_t)(b * 8 + kvh) * 2048 + s * 512) * 256;
  const int h = kvh * 4 + s;
  constexpr float C2 = 0.088388347648318440f * 1.4426950408889634f;
  const int hh = (cb * 4 + s) & 7, kh = cb >> 1;
#define B0() do { SB(); asm volatile("s_waitcnt lgkmcnt(0)" ::: "memory"); __builtin_amdgcn_s_barrier(); CB(); SB(); } while (0)
  if (__builtin_expect(w >= 4, 1)) {
    const unsigned v0 = g * 256 + ((c ^ g) << 4), v1 = v0 ^ 64u, v2 = v0 ^ 128u, v3 = v0 ^ 192u;
    const unsigned lb = (unsigned)(uintptr_t)(L + L_K) + (w - 4) * 4096;
    const size_t off = kvbase + (w - 4) * 4096;
    ARGS();
    _Pragma("nounroll") for (int r = 0; r < 16; ++r) {
      if (r >= 7) lds_wait<1>(cnt + C_KC + (r - 8), r == 7 ? 4u : 2u);
      dma_tile((r < 8 ? (const unsigned char*)kc : (const unsigned char*)vc) + off + (r & 7) * 16384, v0, v1, v2, v3, lb + (r & 7) * 16384);
      if (r == 2) B0();
      if (r >= 2) { waitvm<8>(); lds_signal(cnt + C_R + r - 2); }
    }
    waitvm<4>(); lds_signal(cnt + C_R + 14);
    waitvm<0>(); lds_signal(cnt + C_R + 15);
  }
  CB();
  if (__builtin_expect(w < 4, 1)) {
    v4u xf[2], wr[8];
    unsigned ep;
    unsigned char* wsp = ws;
    {
      const int k0 = 256 * kh + 64 * w + 32 * (c >> 3) + 8 * g;
      ARGS();
      const uint16_t* p = wq + (size_t)k0 * 4096 + kvh * 512 + hh * 64 + 8 * (c & 7);
#pragma unroll
      for (int i = 0; i < 8; ++i) wr[i] = *(const GAS v4u*)(p + (size_t)i * 4096);
#pragma unroll
      for (int e = 0; e < 2; ++e) xf[e] = *(const GAS v4u*)(x + (size_t)(16 * G + 8 * e + (c & 7)) * 512 + k0);
      asm volatile("" : "+s"(wsp));
      if (tid < 64) cnt[tid] = 0u;
    }
    const __amdgpu_buffer_rsrc_t rs = __builtin_amdgcn_make_buffer_rsrc((void*)wsp, 0, WS_END, 0x00020000);
    ep = __builtin_amdgcn_raw_buffer_load_b32(rs, WS_EPOCH + G * 256, 0, 16);
    B0();
    {
      v4u fr[8];
      transpose8(wr, fr);
      LAS unsigned char* p = L + L_QP + ((w * 2 + (g >> 1)) * 16 + 4 * (g & 1)) * 256 + (c & 7) * 32;
#pragma unroll
      for (int e = 0; e < 2; ++e) {
        f32x4 acc[8];
#pragma unroll
        for (int j = 0; j < 8; ++j) acc[j] = mfma(xf[e], fr[j], f32x4{0.f, 0.f, 0.f, 0.f});
        if ((g >> 1) == (c >> 3)) {
#pragma unroll
          for (int r = 0; r < 4; ++r) {
            *(LAS f32x4*)(p + (8 * e + r) * 256) = f32x4{acc[0][r], acc[1][r], acc[2][r], acc[3][r]};
            *(LAS f32x4*)(p + (8 * e + r) * 256 + 16) = f32x4{acc[4][r], acc[5][r], acc[6][r], acc[7][r]};
          }
        }
      }
    }
    lds_signal(cnt + C_QP);
    const unsigned tag = __builtin_amdgcn_readfirstlane(ep) + 1u;
    if (tid == 0) cnt[C_TAG] = tag;
    lds_wait<0>(cnt + C_QP, 4u);
    {
      const int r = tid >> 4, nq = tid & 15;
      f32x4 sum = {0.f, 0.f, 0.f, 0.f};
#pragma unroll
      for (int p = 0; p < 8; ++p) sum += *(const LAS f32x4*)(L + L_QP + (p * 16 + r) * 256 + nq * 16);
      lds_signal(cnt + C_QR);
      const v4u unit = {cvtpk(sum[0], sum[1]), tag, cvtpk(sum[2], sum[3]), tag};
      const int row = (hh >> 1) * 4 + (r & 3), dq = (hh & 1) * 16 + nq;
      __builtin_amdgcn_raw_buffer_store_b128(unit, rs, WS_Q + (((4 * G + w) * 8 + kvh) * 2 + kh) * 8192 + (row * 32 + dq) * 16, 0, 16);
    }
    const int opub = WS_O + (G * 32 + kvh * 4 + (c >> 2)) * 16384 + (cb * 4 + s) * 1024 + w * 256 + (c & 3) * 64 + g * 16;
    const int ypub = WS_Y + ((G * 4 + g) * 32 + 8 * cb + 2 * w + (c & 1)) * 4096 + h * 128 + (c >> 1) * 16;
    v4u kf[32], qt[4];
    const int qo = WS_Q + (b * 8 + kvh) * 16384 + tid * 16;
#pragma unroll
    for (int j = 0; j < 4; ++j) {
      if (j == 2) {
        __builtin_amdgcn_raw_buffer_store_b128(v4u{EMPTY, EMPTY, EMPTY, EMPTY}, rs, opub, 0, 16);
        __builtin_amdgcn_raw_buffer_store_b128(v4u{EMPTY, EMPTY, EMPTY, EMPTY}, rs, ypub, 0, 16);
        SB();
      }
      if (j == 3) {
#pragma unroll
        for (int i = 0; i < 4; ++i) qt[i] = __builtin_amdgcn_raw_buffer_load_b128(rs, qo + i * 4096, 0, 16);
        SB();
      }
      lds_wait<1>(cnt + C_R + 2 * j + (w >> 1), 4u);
#pragma unroll
      for (int t = 0; t < 2; ++t)
#pragma unroll
        for (int stp = 0; stp < 4; ++stp) kf[(2 * j + t) * 4 + stp] = *(const LAS v4u*)(L + L_K + (8 * j + 2 * w + t) * 4096 + c * 256 + (((4 * stp + g) ^ c) << 4));
      lds_signal(cnt + C_KC + 2 * j + (w >> 1));
    }
    {
      v2u u[4]; int o[4];
#pragma unroll
      for (int i = 0; i < 4; ++i) { u[i] = v2u{qt[i][0], qt[i][2]}; o[i] = ((qt[i][1] != tag) | (qt[i][3] != tag)) ? qo + i * 4096 : OOB; }
      poll_units<4, 2>(rs, o, tag, u);
#pragma unroll
      for (int i = 0; i < 2; ++i)
        *(LAS v2u*)(L + L_QI + ((tid >> 5) + 8 * i) * 272 + (tid & 31) * 8) =
            v2u{cvtpk(bflo(u[i][0]) + bflo(u[i + 2][0]), bfhi(u[i][0]) + bfhi(u[i + 2][0])), cvtpk(bflo(u[i][1]) + bflo(u[i + 2][1]), bfhi(u[i][1]) + bfhi(u[i + 2][1]))};
    }
    lds_signal(cnt + C_QI);
    waitvm<0>();
    lds_wait<0>(cnt + C_QI, 4u);
    v4u qf[4];
#pragma unroll
    for (int stp = 0; stp < 4; ++stp) qf[stp] = *(const LAS v4u*)(L + L_QI + c * 272 + stp * 64 + g * 16);
    f32x4 sc[8];
#pragma unroll
    for (int t = 0; t < 8; ++t) sc[t] = f32x4{0.f, 0.f, 0.f, 0.f};
#pragma unroll
    for (int stp = 0; stp < 4; ++stp)
#pragma unroll
      for (int t = 0; t < 8; ++t) sc[t] = mfma(kf[t * 4 + stp], qf[stp], sc[t]);
    float m = sc[0][0];
#pragma unroll
    for (int t = 0; t < 8; ++t)
#pragma unroll
      for (int r = 0; r < 4; ++r) m = fmaxf(m, sc[t][r]);
    m = xor32(xor16(m, true), true);
    if (g == 0) *(LAS float*)(L + L_MAX + (w * 16 + c) * 4) = m;
    lds_signal(cnt + C_MAX);
    lds_wait<0>(cnt + C_MAX, 4u);
#pragma unroll
    for (int ww = 0; ww < 4; ++ww) m = fmaxf(m, *(const LAS float*)(L + L_MAX + (ww * 16 + c) * 4));
    const float ms = m * C2;
    float l0 = 0.f, l1 = 0.f;
#pragma unroll
    for (int t = 0; t < 8; ++t) {
#pragma unroll
      for (int r = 0; r < 4; ++r) sc[t][r] = __builtin_amdgcn_exp2f(fmaf(sc[t][r], C2, -ms));
      l0 += sc[t][0] + sc[t][1]; l1 += sc[t][2] + sc[t][3];
    }
#pragma unroll
    for (int j = 0; j < 4; ++j) {
      const v4u pf = {cvtpk(sc[2 * j][0], sc[2 * j][1]), cvtpk(sc[2 * j][2], sc[2 * j][3]), cvtpk(sc[2 * j + 1][0], sc[2 * j + 1][1]), cvtpk(sc[2 * j + 1][2], sc[2 * j + 1][3])};
      *(LAS v4u*)(L + L_P + (4 * j + w) * 1024 + lane * 16) = pf;
    }
    const float l = xor32(xor16(l0 + l1, false), false);
    if (g == 0) *(LAS float*)(L + L_SUM + (w * 16 + c) * 4) = l;
    lds_signal(cnt + C_P);
    lds_wait<0>(cnt + C_P, 4u);
    v4u pf[16];
#pragma unroll
    for (int i = 0; i < 16; ++i) pf[i] = *(const LAS v4u*)(L + L_P + i * 1024 + lane * 16);
    if (w == 0) {
      float L4 = 0.f;
#pragma unroll
      for (int ww = 0; ww < 4; ++ww) L4 += *(const LAS float*)(L + L_SUM + (ww * 16 + c) * 4);
      __builtin_amdgcn_raw_buffer_store_b64(v2u{tag, tag}, rs, lane == 0 ? WS_F2 + ((G * 4 + cb) * 32 + h) * 128 : OOB, 0, 16);
      const v4u unit = {__float_as_uint(ms), tag, __float_as_uint(L4), tag};
      __builtin_amdgcn_raw_buffer_store_b128(unit, rs, g == 0 ? WS_STAT + (G * 32 + kvh * 4 + (c >> 2)) * 1024 + ((4 * cb + (c & 3)) * 4 + s) * 16 : OOB, 0, 16);
    }
    v4u wf[8], stv;
    const int so = WS_STAT + (G * 32 + h) * 1024 + (w * 16 + c) * 16;
    f32x4 oa[2] = {f32x4{0.f, 0.f, 0.f, 0.f}, f32x4{0.f, 0.f, 0.f, 0.f}};
    {
      const int key = 4 * g + (c >> 2);
      LAS unsigned char* vb0 = L + L_K + key * 256 + (((4 * w + ((c >> 1) & 1)) ^ key) << 4) + (c & 1) * 8;
      LAS unsigned char* vb1 = L + L_K + key * 256 + (((4 * w + 2 + ((c >> 1) & 1)) ^ key) << 4) + (c & 1) * 8;
#pragma unroll
      for (int r = 0; r < 8; ++r) {
        if (r < 6) lds_wait<1>(cnt + C_R + 8 + r, 4u); else lds_wait<0>(cnt + C_R + 8 + r, 4u);
        v2u a0[4], a1[4];
#pragma unroll
        for (int t = 0; t < 4; ++t) { a0[t] = trread(vb0 + (4 * r + t) * 4096); a1[t] = trread(vb1 + (4 * r + t) * 4096); }
#pragma unroll
        for (int bl = 0; bl < 2; ++bl) {
          oa[0] = mfma(v4u{a0[2 * bl][0], a0[2 * bl][1], a0[2 * bl + 1][0], a0[2 * bl + 1][1]}, pf[2 * r + bl], oa[0]);
          oa[1] = mfma(v4u{a1[2 * bl][0], a1[2 * bl][1], a1[2 * bl + 1][0], a1[2 * bl + 1][1]}, pf[2 * r + bl], oa[1]);
        }
        if (r == 2) lds_signal(cnt + C_PVH);
        if (r == 5) {
          const uint16_t* wop = wo + (size_t)(h * 128 + 32 * w + 4 * g) * 512 + 128 * cb + 8 * c;
#pragma unroll
          for (int i = 0; i < 8; ++i) wf[i] = *(const GAS v4u*)(wop + (size_t)(16 * (i >> 2) + (i & 3)) * 512);
          stv = __builtin_amdgcn_raw_buffer_load_b128(rs, so, 0, 16);
          SB();
        }
      }
    }
    __builtin_amdgcn_raw_buffer_store_b128(v4u{cvtpay(oa[0][0], oa[0][1]), cvtpay(oa[0][2], oa[0][3]), cvtpay(oa[1][0], oa[1][1]), cvtpay(oa[1][2], oa[1][3])}, rs, opub, 0, 16);
    SB();
    v4u bw[2][4];
    {
      v4u wt[8];
      transpose8(wf, wt);
      lds_wait<0>(cnt + C_PVH, 4u);
#pragma unroll
      for (int j = 0; j < 8; ++j) *(LAS v4u*)(L + L_K + (j * 4 + w) * 1056 + lane * 16) = wt[j];
      lds_signal(cnt + C_W);
      lds_wait<0>(cnt + C_W, 4u);
#pragma unroll
      for (int mm = 0; mm < 2; ++mm)
#pragma unroll
        for (int u = 0; u < 4; ++u) bw[mm][u] = *(const LAS v4u*)(L + L_K + ((c & 7) * 4 + u) * 1056 + (g * 16 + 2 * (2 * w + mm) + (c >> 3)) * 16);
    }
    {
      int o1[1] = {((stv[1] != tag) | (stv[3] != tag)) ? so : OOB}; v2u u1[1] = {v2u{stv[0], stv[2]}};
      poll_units<1, 0>(rs, o1, tag, u1);
      const float mo = __uint_as_float(u1[0][0]), lo = __uint_as_float(u1[0][1]);
      float M = fmaxf(mo, dppf<0xB1>(mo)); M = fmaxf(M, dppf<0x4E>(M));
      const float e = __builtin_amdgcn_exp2f(mo - M);
      float Lt = e * lo; Lt += dppf<0xB1>(Lt); Lt += dppf<0x4E>(Lt);
      const float wv = e * __builtin_amdgcn_rcpf(Lt);
      const float wg[4] = {dppf<0x00>(wv), dppf<0x55>(wv), dppf<0xAA>(wv), dppf<0xFF>(wv)};
      __builtin_amdgcn_s_sleep(3);
      v4u t[4];
      poll_filled(rs, WS_O + (G * 32 + h) * 16384 + w * 4096 + lane * 16, t);
      v4u o4;
#pragma unroll
      for (int k = 0; k < 4; ++k) {
        float a0 = 0.f, a1 = 0.f;
#pragma unroll
        for (int sr = 0; sr < 4; ++sr) { a0 = fmaf(wg[sr], bflo(t[sr][k]), a0); a1 = fmaf(wg[sr], bfhi(t[sr][k]), a1); }
        o4[k] = cvtpk(a0, a1);
      }
      *(LAS v4u*)(L + L_AI + (4 * w + (c >> 2)) * 272 + g * 64 + (c & 3) * 16) = o4;
    }
    lds_signal(cnt + C_AI);
    lds_wait<0>(cnt + C_AI, 4u);
    {
      v4u af[4];
#pragma unroll
      for (int u2 = 0; u2 < 4; ++u2) af[u2] = *(const LAS v4u*)(L + L_AI + c * 272 + u2 * 64 + g * 16);
      f32x4 y0 = {0.f, 0.f, 0.f, 0.f}, y1 = {0.f, 0.f, 0.f, 0.f};
#pragma unroll
      for (int u2 = 0; u2 < 4; ++u2) { y0 = mfma(af[u2], bw[0][u2], y0); y1 = mfma(af[u2], bw[1][u2], y1); }
      const unsigned p00 = cvtpay(y0[0], y0[1]), p01 = cvtpay(y0[2], y0[3]), p10 = cvtpay(y1[0], y1[1]), p11 = cvtpay(y1[2], y1[3]);
      const bool odd = c & 1;
      const unsigned r0 = __builtin_amdgcn_mov_dpp(odd ? p00 : p10, 0xB1, 0xf, 0xf, true), r1 = __builtin_amdgcn_mov_dpp(odd ? p01 : p11, 0xB1, 0xf, 0xf, true);
      __builtin_amdgcn_raw_buffer_store_b128(odd ? v4u{r0, r1, p10, p11} : v4u{p00, p01, r0, r1}, rs, ypub, 0, 16);
    }
    lds_signal(cnt + C_Y);
  }
  CB();
  {
    if (w == 4) {
      unsigned char* wsp = ws;
      asm volatile("" : "+s"(wsp));
      const __amdgpu_buffer_rsrc_t rs = __builtin_amdgcn_make_buffer_rsrc((void*)wsp, 0, WS_END, 0x00020000);
      lds_wait<2>(cnt + C_R + 15, 4u);
      const unsigned tag = __builtin_amdgcn_readfirstlane(cnt[C_TAG]);
      {
        int fo = lane < 32 ? WS_F2 + ((G * 4 + (h >> 3)) * 32 + lane) * 128 : OOB;
        _Pragma("nounroll") for (unsigned spins = 0; spins < SPIN_CAP; ++spins) {
          CB();
          const v2u f = __builtin_amdgcn_raw_buffer_load_b64(rs, fo, 0, 16);
          fo = ((f[0] == tag) & (f[1] == tag)) ? OOB : fo;
          if (!__any(fo >= 0)) break;
        }
      }
      lds_wait<2>(cnt + C_Y, 4u);
      __builtin_amdgcn_s_sleep(6);
      v4u t[4];
      poll_filled(rs, WS_Y + (b * 32 + h) * 4096 + lane * 16, t);
      float v[8];
#pragma unroll
      for (int k = 0; k < 4; ++k) {
        v[2 * k] = (bflo(t[0][k]) + bflo(t[1][k])) + (bflo(t[2][k]) + bflo(t[3][k]));
        v[2 * k + 1] = (bfhi(t[0][k]) + bfhi(t[1][k])) + (bfhi(t[2][k]) + bfhi(t[3][k]));
      }
      float z[4], q2[2];
#pragma unroll
      for (int k = 0; k < 4; ++k) { const auto sw = __builtin_amdgcn_permlane32_swap(__float_as_uint(v[k]), __float_as_uint(v[k + 4]), false, false); z[k] = __uint_as_float(sw[0]) + __uint_as_float(sw[1]); }
#pragma unroll
      for (int k = 0; k < 2; ++k) { const auto sw = __builtin_amdgcn_permlane16_swap(__float_as_uint(z[k]), __float_as_uint(z[k + 2]), false, false); q2[k] = __uint_as_float(sw[0]) + __uint_as_float(sw[1]); }
      const float e0 = q2[0] + dppf<0x128>(q2[0]), e1 = q2[1] + dppf<0x128>(q2[1]);
      const float y = (lane & 8) ? e1 : e0;
      *(GAS uint16_t*)(out + (size_t)(b * 4 + ((lane >> 3) & 3)) * 512 + 16 * h + 2 * (lane & 7) + (lane >> 5)) = (uint16_t)cvtpk(y, y);
      __builtin_amdgcn_raw_buffer_store_b32(tag, rs, (cb == 0 && h == 0 && lane == 0) ? WS_EPOCH + G * 256 : OOB, 0, 16);
    }
  }
}

extern "C" void kernel_launch(void* const* d_in, const int* in_sizes, int n_in, void* d_out, int out_size, void* d_ws, size_t ws_size,
                              hipStream_t stream) {
  (void)hipFuncSetAttribute((const void*)gqa_fused, hipFuncAttributeMaxDynamicSharedMemorySize, L_END);
  gqa_fused<<<256, 512, L_END, stream>>>((const uint16_t*)d_in[0], (const uint16_t*)d_in[1], (const uint16_t*)d_in[2], (const uint16_t*)d_in[3],
                                         (const uint16_t*)d_in[4], (uint16_t*)d_out, (unsigned char*)d_ws);
}
```
